```python
import torch

def _softmax(x, axis=-1):
    return torch.softmax(x, dim=axis)
def _gelu(x):
    return torch.nn.functional.gelu(x, approximate="tanh")
def _silu(x):
    return torch.nn.functional.silu(x)
def _sigmoid(x):
    return torch.sigmoid(x)
def _layernorm(x, axis=-1, eps=1e-5):
    m = x.mean(dim=axis, keepdim=True)
    v = x.var(dim=axis, keepdim=True, unbiased=False)
    return (x - m) / torch.sqrt(v + eps)
def _scatter_add(tgt, idx, src, axis=0):
    return tgt.clone().index_add_(axis, idx, src)
def _segment_sum(src, seg, n):
    out = torch.zeros((n,) + src.shape[1:], dtype=src.dtype)
    return out.index_add_(0, seg, src)
def _pad(x, pw):
    flat = tuple(v for pair in reversed(pw) for v in pair)
    return torch.nn.functional.pad(x, flat)

kernel_name = 'sequential_preLN__linear_kernel_hedgehog__glu_gated_glu__prompt'

def setup_inputs():

    _w = (lambda a: (lambda t: t.to(torch.bfloat16) if t.is_floating_point() else t)(torch.as_tensor(a)))
    def _A_setup():
        B, S, D, H = 1, 2048, 448, 16
        Dh = D // H
        return {
            "x": _w((lambda *s: torch.randn(*s, dtype=torch.bfloat16) * 0.05)(B, S, D)),
            "wqkv": _w((lambda *s: torch.randn(*s, dtype=torch.bfloat16) * 0.2)(D, 3 * D)),
            "w_phi": _w((lambda *s: torch.randn(*s, dtype=torch.bfloat16) * 0.2)(H, Dh, Dh) * (Dh ** -0.5)),
            "wo": _w((lambda *s: torch.randn(*s, dtype=torch.bfloat16) * 0.05)(D, D)),
        }
    _A_in = _A_setup()

    def _B_setup():
        B, S, D, F = 1, 2048, 448, 1792
        return {
            "x": _w((lambda *s: torch.randn(*s, dtype=torch.bfloat16) * 0.05)(B, S, D)),
            "w_gate": _w((lambda *s: torch.randn(*s, dtype=torch.bfloat16) * 0.05)(D, F)),
            "w_up": _w((lambda *s: torch.randn(*s, dtype=torch.bfloat16) * 0.05)(D, F)),
            "w_down": _w((lambda *s: torch.randn(*s, dtype=torch.bfloat16) * 0.05)(F, D)),
        }
    _B_in = _B_setup()

    return {
        "A_x": _A_in["x"],
        "A_wqkv": _A_in["wqkv"],
        "A_w_phi": _A_in["w_phi"],
        "A_wo": _A_in["wo"],
        "B_x": _B_in["x"],
        "B_w_gate": _B_in["w_gate"],
        "B_w_up": _B_in["w_up"],
        "B_w_down": _B_in["w_down"],
    }

def reference(A_x, A_wqkv, A_w_phi, A_wo, B_x, B_w_gate, B_w_up, B_w_down):
    def _A_ref(x, wqkv, w_phi, wo):
        B, S, D = x.shape
        H = 16; Dh = D // H
        qkv = (lambda x, *d: x.permute(*d))((x @ wqkv).reshape(B, S, 3, H, Dh), 2, 0, 3, 1, 4)
        q, k, v = qkv[0], qkv[1], qkv[2]
        pq = torch.einsum("bhsd,hdf->bhsf", q, w_phi)
        pk = torch.einsum("bhsd,hdf->bhsf", k, w_phi)
        phi_q = torch.concatenate([_softmax(pq, axis=-1), _softmax(-pq, axis=-1)], axis=-1)
        phi_k = torch.concatenate([_softmax(pk, axis=-1), _softmax(-pk, axis=-1)], axis=-1)
        kv = torch.einsum("bhsf,bhsd->bhfd", phi_k, v)
        z  = torch.einsum("bhsf->bhf", phi_k)
        num = torch.einsum("bhqf,bhfd->bhqd", phi_q, kv)
        den = torch.einsum("bhqf,bhf->bhq", phi_q, z)[..., None] + 1e-6
        o = num / den
        return (lambda x, *d: x.permute(*d))(o, 0, 2, 1, 3).reshape(B, S, D) @ wo

    def _B_ref(x, w_gate, w_up, w_down):
        g = torch.einsum("bsd,df->bsf", x, w_gate)
        u = torch.einsum("bsd,df->bsf", x, w_up)
        h = _sigmoid(g) * u
        return torch.einsum("bsf,fd->bsd", h, w_down)

    x = A_x
    h = x + _A_ref(_layernorm(x, axis=-1), A_wqkv, A_w_phi, A_wo)
    h2 = h + _B_ref(_layernorm(h, axis=-1), B_w_gate, B_w_up, B_w_down)
    return h2
```

```cpp
#include <hip/hip_runtime.h>
#include <hip/hip_bf16.h>
#include <stdint.h>

typedef __attribute__((ext_vector_type(8))) __bf16 bf16x8;
typedef __attribute__((ext_vector_type(4))) __bf16 bf16x4;
typedef __attribute__((ext_vector_type(2))) __bf16 bf16x2;
typedef __attribute__((ext_vector_type(4))) short s16x4;
typedef __attribute__((ext_vector_type(8))) short s16x8;
typedef __attribute__((ext_vector_type(16))) float f32x16;
typedef __attribute__((ext_vector_type(4))) float f32x4;
typedef __attribute__((ext_vector_type(2))) float f32x2;
typedef __attribute__((ext_vector_type(4))) unsigned u32x4;
typedef __attribute__((ext_vector_type(2))) unsigned u32x2;
#define LAS __attribute__((address_space(3)))
#define DI __device__ __forceinline__
#define MFMA(a, b, c) __builtin_amdgcn_mfma_f32_32x32x16_bf16((a), (b), (c), 0, 0, 0)
#define WAIT_V(n) asm volatile("s_waitcnt vmcnt(" #n ")" ::: "memory")
#define WAIT_L0 asm volatile("s_waitcnt lgkmcnt(0)" ::: "memory")
#define BAR __builtin_amdgcn_s_barrier()

constexpr int S_ = 2048, D_ = 448, H_ = 16, F_ = 1792;
constexpr float LOG2E = 1.4426950408889634f;

constexpr size_t WS_KVP = 0;
constexpr size_t WS_PHIQ = WS_KVP + (2u << 20);
constexpr size_t WS_O = WS_PHIQ + (4u << 20);
constexpr size_t WS_HB = WS_O + (2u << 20);
constexpr size_t WS_HH = WS_HB + (2u << 20);

DI void glds16(const void* g, LAS char* l) {
  __builtin_amdgcn_global_load_lds((const unsigned*)g, (LAS unsigned*)l, 16, 0, 0);
}
DI bf16x8 tr_frag(LAS char* p0, LAS char* p1) {
  s16x4 a = __builtin_amdgcn_ds_read_tr16_b64_v4i16((LAS s16x4*)p0);
  s16x4 b = __builtin_amdgcn_ds_read_tr16_b64_v4i16((LAS s16x4*)p1);
  s16x8 r = __builtin_shufflevector(a, b, 0, 1, 2, 3, 4, 5, 6, 7);
  return __builtin_bit_cast(bf16x8, r);
}
DI unsigned pk2(float a, float b) {
  f32x2 v = {a, b};
  bf16x2 r = __builtin_convertvector(v, bf16x2);
  return __builtin_bit_cast(unsigned, r);
}
template <int S>
DI bf16x8 pack8(const f32x16& x) {
  u32x4 p;
  p[0] = pk2(x[8 * S + 0], x[8 * S + 1]);
  p[1] = pk2(x[8 * S + 2], x[8 * S + 3]);
  p[2] = pk2(x[8 * S + 4], x[8 * S + 5]);
  p[3] = pk2(x[8 * S + 6], x[8 * S + 7]);
  return __builtin_bit_cast(bf16x8, p);
}
DI float dot2(unsigned a, unsigned b, float c) {
  return __builtin_amdgcn_fdot2_f32_bf16(__builtin_bit_cast(bf16x2, a), __builtin_bit_cast(bf16x2, b), c, false);
}
constexpr unsigned ONES2 = 0x3F803F80u;
DI float frag_sum(const bf16x8& f, float c) {
  u32x4 u = __builtin_bit_cast(u32x4, f);
  c = dot2(u[0], ONES2, c); c = dot2(u[1], ONES2, c); c = dot2(u[2], ONES2, c); c = dot2(u[3], ONES2, c);
  return c;
}
DI float frag_sumsq(const bf16x8& f, float c) {
  u32x4 u = __builtin_bit_cast(u32x4, f);
  c = dot2(u[0], u[0], c); c = dot2(u[1], u[1], c); c = dot2(u[2], u[2], c); c = dot2(u[3], u[3], c);
  return c;
}
DI void xhalf(float x, float& lo, float& hi) {
  unsigned u = __builtin_bit_cast(unsigned, x);
  auto r = __builtin_amdgcn_permlane32_swap(u, u, false, false);
  lo = __builtin_bit_cast(float, (unsigned)r[0]);
  hi = __builtin_bit_cast(float, (unsigned)r[1]);
}
DI float xsum(float x) { float a, b; xhalf(x, a, b); return a + b; }
DI float xmax(float x) { float a, b; xhalf(x, a, b); return fmaxf(a, b); }
DI float xmin(float x) { float a, b; xhalf(x, a, b); return fminf(a, b); }
DI float bf_lo(unsigned u) { return __builtin_bit_cast(float, u << 16); }
DI float bf_hi(unsigned u) { return __builtin_bit_cast(float, u & 0xffff0000u); }

DI void dual_softmax(const f32x16& p, int hh, f32x16& ep, f32x16& en) {
  float mx = p[0], mn = p[0];
#pragma unroll
  for (int r = 1; r < 12; ++r) { mx = fmaxf(mx, p[r]); mn = fminf(mn, p[r]); }
  float mx2 = mx, mn2 = mn;
#pragma unroll
  for (int r = 12; r < 16; ++r) { mx2 = fmaxf(mx2, p[r]); mn2 = fminf(mn2, p[r]); }
  mx = hh ? mx : mx2; mn = hh ? mn : mn2;
  mx = xmax(mx); mn = xmin(mn);
  const float a = -mx * LOG2E, b = mn * LOG2E;
  float sp = 0.f, sn = 0.f;
#pragma unroll
  for (int r = 0; r < 16; ++r) {
    float e1 = __builtin_amdgcn_exp2f(fmaf(p[r], LOG2E, a));
    float e2 = __builtin_amdgcn_exp2f(fmaf(p[r], -LOG2E, b));
    if (r >= 12) { e1 = hh ? 0.f : e1; e2 = hh ? 0.f : e2; }
    ep[r] = e1; en[r] = e2; sp += e1; sn += e2;
  }
  sp = xsum(sp); sn = xsum(sn);
  const float rp = 1.0f / sp, rn = 1.0f / sn;
#pragma unroll
  for (int r = 0; r < 16; ++r) { ep[r] *= rp; en[r] *= rn; }
}

constexpr int P1_XB = 16384, P1_WB = 12288, P1_SLOT = P1_XB + P1_WB, P1_NSLOT = 5;
constexpr int P1_MISC = P1_SLOT * P1_NSLOT;
constexpr int P1_STAT = P1_MISC;
constexpr int P1_CSP = P1_STAT + 1024;
constexpr int P1_LDS = P1_CSP + 1536;
constexpr int T_PITCH = 200;

__global__ __launch_bounds__(512, 2) void k_p1(const __bf16* __restrict__ x, const __bf16* __restrict__ wqkv,
                                               const __bf16* __restrict__ wphi, char* __restrict__ ws) {
  extern __shared__ __attribute__((aligned(16))) char smem_[];
  LAS char* lds = (LAS char*)smem_;
  const int tid = threadIdx.x, lane = tid & 63;
  const int w = __builtin_amdgcn_readfirstlane(tid >> 6);
  const int bid = blockIdx.x;
  const int m = ((bid & 7) & 3) * 4 + ((bid >> 3) >> 3), h = ((bid & 7) >> 2) * 8 + ((bid >> 3) & 7);
  const int hodd = h & 1;
  const int tt = w & 3;
  const bool isq = w >= 4;
  const int t32 = lane & 31, hh = lane >> 5, blk = (lane >> 4) & 1, q4 = (lane & 15) >> 2, p4 = lane & 3;

  const char* xsrc[2];
#pragma unroll
  for (int i = 0; i < 2; ++i) {
    const int row = 8 * (2 * w + i) + (lane >> 3);
    const int c = (lane & 7) ^ ((row >> 1) & 7);
    xsrc[i] = (const char*)x + (size_t)(m * 128 + row) * (D_ * 2) + c * 16;
  }
  const char* wsrc[2];
  int wq[2];
#pragma unroll
  for (int i = 0; i < 2; ++i) {
    const int q = isq ? (8 + (w - 4)) : (2 * w + i);
    wq[i] = q;
    const int off = q * 1024 + lane * 16;
    const int row = off / 192, rem = off % 192, seg = rem >> 6, c = (rem & 63) >> 4;
    wsrc[i] = (const char*)wqkv + (size_t)row * (3 * D_ * 2) + seg * (D_ * 2) + (56 * h - 8 * hodd) + c * 16;
  }
  auto issue = [&](int c) {
    LAS char* slot = lds + (c % P1_NSLOT) * P1_SLOT;
    glds16(xsrc[0] + c * 128, slot + (2 * w) * 1024);
    glds16(xsrc[1] + c * 128, slot + (2 * w + 1) * 1024);
    glds16(wsrc[0] + (size_t)c * 64 * (3 * D_ * 2), slot + P1_XB + wq[0] * 1024);
    if (!isq) glds16(wsrc[1] + (size_t)c * 64 * (3 * D_ * 2), slot + P1_XB + wq[1] * 1024);
  };
  bf16x8 wph[2];
#pragma unroll
  for (int s = 0; s < 2; ++s)
#pragma unroll
    for (int j = 0; j < 8; ++j) {
      const int d = 16 * s + 8 * (j >> 2) + 4 * hh + (j & 3) - 4 * hodd;
      const bool ok = (d >= 0) && (d < 28) && (t32 < 28);
      __bf16 v = wphi[(h * 28 + (ok ? d : 0)) * 28 + (ok ? t32 : 0)];
      wph[s][j] = ok ? v : (__bf16)0.0f;
    }
#pragma unroll
  for (int c = 0; c < P1_NSLOT; ++c) issue(c);

  f32x16 acc0 = {}, acc1 = {};
  float ssum = 0.f, ssq = 0.f, cs0 = 0.f, cs1 = 0.f;
  const int xrow = tt * 32 + t32;
  const int xoff = xrow * 128;
  const int xsw = (xrow >> 1) & 7;
  const int woff = (8 * hh + q4) * 192 + blk * 32 + p4 * 8 + (isq ? 0 : 64);
#pragma unroll
  for (int c = 0; c < 7; ++c) {
    if (isq) {
      if (c == 0) WAIT_V(12); else if (c <= 3) WAIT_V(9); else if (c == 4) WAIT_V(6); else if (c == 5) WAIT_V(3); else WAIT_V(0);
    } else {
      if (c == 0) WAIT_V(16); else if (c <= 3) WAIT_V(12); else if (c == 4) WAIT_V(8); else if (c == 5) WAIT_V(4); else WAIT_V(0);
    }
    WAIT_L0;
    BAR;
    if (c == 1) issue(5);
    if (c == 2) issue(6);
    LAS char* slot = lds + (c % P1_NSLOT) * P1_SLOT;
#pragma unroll
    for (int s = 0; s < 4; ++s) {
      bf16x8 xb = *(LAS bf16x8*)(slot + xoff + (((2 * s + hh) ^ xsw) * 16));
      LAS char* wp = slot + P1_XB + woff + s * 16 * 192;
      bf16x8 a0 = tr_frag(wp, wp + 4 * 192);
      acc0 = MFMA(a0, xb, acc0);
      if (s == tt) cs0 = frag_sum(a0, cs0);
      if (!isq) {
        bf16x8 a1 = tr_frag(wp + 64, wp + 64 + 4 * 192);
        acc1 = MFMA(a1, xb, acc1);
        if (s == tt) cs1 = frag_sum(a1, cs1);
      } else {
        ssum = frag_sum(xb, ssum);
        ssq = frag_sumsq(xb, ssq);
      }
    }
  }
  cs0 = xsum(cs0); cs1 = xsum(cs1);
  LAS float* csp = (LAS float*)(lds + P1_CSP);
  LAS float* stat = (LAS float*)(lds + P1_STAT);
  if (isq) {
    ssum = xsum(ssum); ssq = xsum(ssq);
    const float mu = ssum * (1.0f / D_);
    const float var = fmaxf(ssq * (1.0f / D_) - mu * mu, 0.f);
    const float rstd = 1.0f / sqrtf(var + 1e-5f);
    if (hh == 0) { stat[2 * xrow] = mu; stat[2 * xrow + 1] = rstd; csp[tt * 96 + t32] = cs0; }
  } else if (hh == 0) {
    csp[tt * 96 + 32 + t32] = cs0;
    csp[tt * 96 + 64 + t32] = cs1;
  }
  WAIT_L0;
  BAR;
  const float mu = stat[2 * xrow], rstd = stat[2 * xrow + 1];
  {
    const int seg0 = isq ? 0 : 32;
#pragma unroll
    for (int g = 0; g < 4; ++g) {
      f32x4 c0 = {}, c1 = {};
#pragma unroll
      for (int i = 0; i < 4; ++i) {
        c0 += *(LAS f32x4*)(csp + i * 96 + seg0 + 8 * g + 4 * hh);
        if (!isq) c1 += *(LAS f32x4*)(csp + i * 96 + 64 + 8 * g + 4 * hh);
      }
#pragma unroll
      for (int r = 0; r < 4; ++r) {
        acc0[4 * g + r] = rstd * (acc0[4 * g + r] - mu * c0[r]);
        if (!isq) acc1[4 * g + r] = rstd * (acc1[4 * g + r] - mu * c1[r]);
      }
    }
  }
  f32x16 pq = {};
  pq = MFMA(wph[0], pack8<0>(acc0), pq);
  pq = MFMA(wph[1], pack8<1>(acc0), pq);
  f32x16 ep, en;
  dual_softmax(pq, hh, ep, en);
  LAS char* T = lds;
  if (isq) {
    bf16x8* dst = (bf16x8*)(ws + WS_PHIQ + ((size_t)((m * 4 + tt) * 16 + h) * 4) * 1024) + lane;
    dst[0] = pack8<0>(ep); dst[64] = pack8<1>(ep); dst[128] = pack8<0>(en); dst[192] = pack8<1>(en);
  } else {
    if (hodd) { if (hh == 0) acc1[0] = 1.0f; } else { if (hh == 1) acc1[12] = 1.0f; }
    LAS char* tp = T + xrow * T_PITCH + 8 * hh;
#pragma unroll
    for (int g = 0; g < 4; ++g) {
      u32x2 a = {pk2(ep[4 * g], ep[4 * g + 1]), pk2(ep[4 * g + 2], ep[4 * g + 3])};
      u32x2 b = {pk2(en[4 * g], en[4 * g + 1]), pk2(en[4 * g + 2], en[4 * g + 3])};
      u32x2 c = {pk2(acc1[4 * g], acc1[4 * g + 1]), pk2(acc1[4 * g + 2], acc1[4 * g + 3])};
      *(LAS u32x2*)(tp + 16 * g) = a;
      *(LAS u32x2*)(tp + 64 + 16 * g) = b;
      *(LAS u32x2*)(tp + 128 + 16 * g) = c;
    }
  }
  WAIT_L0;
  BAR;
  if (w < 2) {
    f32x16 kv = {};
    LAS char* ap = T + (8 * hh + q4) * T_PITCH + blk * 32 + p4 * 8;
#pragma unroll
    for (int s = 0; s < 8; ++s) {
      LAS char* p = ap + s * 16 * T_PITCH;
      bf16x8 a = tr_frag(p + w * 64, p + w * 64 + 4 * T_PITCH);
      bf16x8 b = tr_frag(p + 128, p + 128 + 4 * T_PITCH);
      kv = MFMA(a, b, kv);
    }
    f32x4* dst = (f32x4*)(ws + WS_KVP + ((size_t)(h * 16 + m) * 8 + w * 4) * 1024) + lane;
#pragma unroll
    for (int g = 0; g < 4; ++g) { f32x4 v = {kv[4 * g], kv[4 * g + 1], kv[4 * g + 2], kv[4 * g + 3]}; dst[g * 64] = v; }
  }
}

__global__ __launch_bounds__(512, 2) void k_p2(char* __restrict__ ws) {
  extern __shared__ __attribute__((aligned(16))) char smem_[];
  LAS char* lds = (LAS char*)smem_;
  const int tid = threadIdx.x, lane = tid & 63;
  const int w = __builtin_amdgcn_readfirstlane(tid >> 6);
  const int bid = blockIdx.x;
  const int m = ((bid & 7) & 3) * 4 + ((bid >> 3) >> 3), h = ((bid & 7) >> 2) * 8 + ((bid >> 3) & 7);
  const int hodd = h & 1, hh = lane >> 5;
  const f32x4* src = (const f32x4*)(ws + WS_KVP + ((size_t)(h * 16) * 8 + w) * 1024) + lane;
  f32x4 s = {};
#pragma unroll
  for (int i = 0; i < 16; ++i) s += src[(size_t)i * 8 * 64];
  u32x2 pk = {pk2(s[0], s[1]), pk2(s[2], s[3])};
  *(LAS u32x2*)(lds + (w >> 1) * 1024 + lane * 16 + (w & 1) * 8) = pk;
  __syncthreads();
  if (w >= 4) {
    const int tt = w - 4;
    const bf16x8* pq = (const bf16x8*)(ws + WS_PHIQ + ((size_t)((m * 4 + tt) * 16 + h) * 4) * 1024) + lane;
    f32x16 num = {};
#pragma unroll
    for (int i = 0; i < 4; ++i) num = MFMA(*(LAS bf16x8*)(lds + i * 1024 + lane * 16), pq[i * 64], num);
    float lo, hi;
    xhalf(hodd ? num[0] : num[12], lo, hi);
    const float den = (hodd ? lo : hi) + 1e-6f;
    const float rd = 1.0f / den;
#pragma unroll
    for (int r = 0; r < 16; ++r) num[r] *= rd;
    if (hodd) { if (hh == 0) { num[0] = 0.f; num[1] = 0.f; num[2] = 0.f; num[3] = 0.f; } }
    else { if (hh == 1) { num[12] = 0.f; num[13] = 0.f; num[14] = 0.f; num[15] = 0.f; } }
    bf16x8* dst = (bf16x8*)(ws + WS_O + ((size_t)((m * 4 + tt) * 16 + h) * 2) * 1024) + lane;
    dst[0] = pack8<0>(num); dst[64] = pack8<1>(num);
  }
}

DI int wrow_swz(int row) { return ((row >> 1) & 1) << 2; }

DI void reduce4(LAS char* red, const f32x16& a0, const f32x16& a1, int kq, int tt, int lane, float (&o)[8]) {
#pragma unroll
  for (int p = 0; p < 4; ++p) {
    if (p != kq) {
      const f32x16& a = (p >> 1) ? a1 : a0;
      const int rb = (p & 1) * 8;
      f32x4 v0 = {a[rb], a[rb + 1], a[rb + 2], a[rb + 3]}, v1 = {a[rb + 4], a[rb + 5], a[rb + 6], a[rb + 7]};
      LAS char* d = red + (((tt * 4 + p) * 4 + kq) * 2) * 1024 + lane * 16;
      *(LAS f32x4*)d = v0; *(LAS f32x4*)(d + 1024) = v1;
    }
  }
#pragma unroll
  for (int p = 0; p < 4; ++p)
    if (p == kq) {
      const f32x16& a = (p >> 1) ? a1 : a0;
      const int rb = (p & 1) * 8;
#pragma unroll
      for (int i = 0; i < 8; ++i) o[i] = a[rb + i];
    }
  WAIT_L0;
  BAR;
#pragma unroll
  for (int sq = 0; sq < 4; ++sq) {
    if (sq != kq) {
      LAS char* d = red + (((tt * 4 + kq) * 4 + sq) * 2) * 1024 + lane * 16;
      f32x4 v0 = *(LAS f32x4*)d, v1 = *(LAS f32x4*)(d + 1024);
#pragma unroll
      for (int i = 0; i < 4; ++i) { o[i] += v0[i]; o[4 + i] += v1[i]; }
    }
  }
}

__global__ __launch_bounds__(512, 2) void k_p3(const __bf16* __restrict__ x, const __bf16* __restrict__ wo, char* __restrict__ ws) {
  extern __shared__ __attribute__((aligned(16))) char smem_[];
  LAS char* lds = (LAS char*)smem_;
  const int tid = threadIdx.x, lane = tid & 63;
  const int w = __builtin_amdgcn_readfirstlane(tid >> 6);
  const int bid = blockIdx.x;
  const int mp = (bid & 7) * 4 + ((bid >> 3) & 3), jn = bid >> 5;
  const int kq = w & 3, tt = w >> 2;
  const int t32 = lane & 31, hh = lane >> 5, blk = (lane >> 4) & 1, q4 = (lane & 15) >> 2, p4 = lane & 3;
#pragma unroll
  for (int i = 0; i < 7; ++i) {
    const int q = w * 7 + i, row = 8 * q + (lane >> 3), c = (lane & 7) ^ wrow_swz(row);
    glds16((const char*)wo + (size_t)row * (D_ * 2) + jn * 128 + c * 16, lds + q * 1024);
  }
  const int ttg = mp * 2 + tt;
  const bf16x8* osrc = (const bf16x8*)(ws + WS_O + ((size_t)(ttg * 16 + kq * 4) * 2) * 1024) + lane;
  bf16x8 ob[8];
#pragma unroll
  for (int i = 0; i < 8; ++i) ob[i] = osrc[i * 64];
  const char* xr = (const char*)x + (size_t)(ttg * 32 + t32) * (D_ * 2) + (jn * 64 + kq * 16 + 4 * hh) * 2;
  const u32x2 x0 = *(const u32x2*)xr, x1 = *(const u32x2*)(xr + 16);
  WAIT_V(0);
  BAR;
  f32x16 a0 = {}, a1 = {};
#pragma unroll
  for (int i = 0; i < 8; ++i) {
    const int hd = kq * 4 + (i >> 1);
    const int row = 28 * hd - 4 * (hd & 1) + 16 * (i & 1) + 4 * hh + q4;
    const int sw = wrow_swz(row);
    LAS char* p = lds + row * 128 + 8 * (p4 & 1);
    const int c0 = 2 * blk + (p4 >> 1);
    bf16x8 w0 = tr_frag(p + ((c0 ^ sw) * 16), p + 8 * 128 + ((c0 ^ sw) * 16));
    bf16x8 w1 = tr_frag(p + (((c0 + 4) ^ sw) * 16), p + 8 * 128 + (((c0 + 4) ^ sw) * 16));
    a0 = MFMA(w0, ob[i], a0);
    a1 = MFMA(w1, ob[i], a1);
  }
  float o[8];
  reduce4(lds + 57344, a0, a1, kq, tt, lane, o);
  o[0] += bf_lo(x0[0]); o[1] += bf_hi(x0[0]); o[2] += bf_lo(x0[1]); o[3] += bf_hi(x0[1]);
  o[4] += bf_lo(x1[0]); o[5] += bf_hi(x1[0]); o[6] += bf_lo(x1[1]); o[7] += bf_hi(x1[1]);
  u32x4 hv = {pk2(o[0], o[1]), pk2(o[2], o[3]), pk2(o[4], o[5]), pk2(o[6], o[7])};
  *((u32x4*)(ws + WS_HB + ((size_t)ttg * 28 + jn * 4 + kq) * 1024) + lane) = hv;
}

constexpr int P4_HB = 16384, P4_WB = 28672, P4_SLOT = P4_HB + P4_WB, P4_NSLOT = 3;
constexpr int P4_CSP = P4_SLOT * P4_NSLOT;
constexpr int P4_LDS = P4_CSP + 3584;

template <int T0, int NT>
DI void p4_body(LAS char* lds, const char* (&wsrc)[4], const char* hsrc, int w, int lane, int m, int j, char* ws) {
  const int tt = w & 3;
  const int t32 = lane & 31, hh = lane >> 5, blk = (lane >> 4) & 1, q4 = (lane & 15) >> 2, p4 = lane & 3;
  constexpr int NW = (T0 == 0) ? 4 : 3;
  auto issue = [&](int c) {
    LAS char* slot = lds + (c % P4_NSLOT) * P4_SLOT;
    glds16(hsrc + (size_t)c * 4096, slot + (2 * w) * 1024);
    glds16(hsrc + (size_t)c * 4096 + 1024, slot + (2 * w + 1) * 1024);
#pragma unroll
    for (int i = 0; i < NW; ++i) {
      const int q = (T0 == 0) ? (4 * w + i) : (16 + 3 * (w - 4) + i);
      glds16(wsrc[i] + (size_t)c * 64 * (F_ * 2), slot + P4_HB + q * 1024);
    }
  };
  issue(0); issue(1); issue(2);
  f32x16 acc[NT];
#pragma unroll
  for (int i = 0; i < NT; ++i) acc[i] = (f32x16){};
  float cs[NT];
#pragma unroll
  for (int i = 0; i < NT; ++i) cs[i] = 0.f;
  float ssum = 0.f, ssq = 0.f;
  const int woff = (4 * hh + q4) * 448 + blk * 224 + p4 * 8 + T0 * 32;
#pragma unroll
  for (int c = 0; c < 7; ++c) {
    constexpr int PER = NW + 2;
    if (c == 0) { if (PER == 6) WAIT_V(12); else WAIT_V(10); }
    else if (c < 6) { if (PER == 6) WAIT_V(6); else WAIT_V(5); }
    else WAIT_V(0);
    WAIT_L0;
    BAR;
    if (c >= 1 && c + 2 < 7) issue(c + 2);
    LAS char* slot = lds + (c % P4_NSLOT) * P4_SLOT;
#pragma unroll
    for (int s = 0; s < 4; ++s) {
      bf16x8 hb = *(LAS bf16x8*)(slot + (tt * 4 + s) * 1024 + lane * 16);
      LAS char* wp = slot + P4_HB + woff + s * 16 * 448;
#pragma unroll
      for (int i = 0; i < NT; ++i) {
        bf16x8 a = tr_frag(wp + i * 32, wp + i * 32 + 8 * 448);
        acc[i] = MFMA(a, hb, acc[i]);
        if (s == tt) cs[i] = frag_sum(a, cs[i]);
      }
      ssum = frag_sum(hb, ssum);
      ssq = frag_sumsq(hb, ssq);
    }
  }
  LAS float* csp = (LAS float*)(lds + P4_CSP);
#pragma unroll
  for (int i = 0; i < NT; ++i) { float c = xsum(cs[i]); if (hh == 0) csp[(tt * 7 + T0 + i) * 32 + t32] = c; }
  ssum = xsum(ssum); ssq = xsum(ssq);
  const float mu = ssum * (1.0f / D_);
  const float var = fmaxf(ssq * (1.0f / D_) - mu * mu, 0.f);
  const float rstd = 1.0f / sqrtf(var + 1e-5f);
  WAIT_L0;
  BAR;
  bf16x8* dst = (bf16x8*)(ws + WS_HH + ((size_t)(m * 4 + tt) * 112 + j * 7 + T0) * 1024) + lane;
#pragma unroll
  for (int i = 0; i < NT; ++i) {
    float v[16];
#pragma unroll
    for (int g = 0; g < 4; ++g) {
      f32x4 c = {};
#pragma unroll
      for (int k = 0; k < 4; ++k) c += *(LAS f32x4*)(csp + (k * 7 + T0 + i) * 32 + 8 * g + 4 * hh);
#pragma unroll
      for (int r = 0; r < 4; ++r) v[4 * g + r] = rstd * (acc[i][4 * g + r] - mu * c[r]);
    }
    float o[8];
#pragma unroll
    for (int r = 0; r < 8; ++r) {
      const float e = __builtin_amdgcn_exp2f(-v[r] * LOG2E);
      o[r] = v[8 + r] * __builtin_amdgcn_rcpf(1.0f + e);
    }
    u32x4 pk = {pk2(o[0], o[1]), pk2(o[2], o[3]), pk2(o[4], o[5]), pk2(o[6], o[7])};
    dst[i * 64] = __builtin_bit_cast(bf16x8, pk);
  }
}

__global__ __launch_bounds__(512, 2) void k_p4(const __bf16* __restrict__ wg, const __bf16* __restrict__ wu, char* __restrict__ ws) {
  extern __shared__ __attribute__((aligned(16))) char smem_[];
  LAS char* lds = (LAS char*)smem_;
  const int tid = threadIdx.x, lane = tid & 63;
  const int w = __builtin_amdgcn_readfirstlane(tid >> 6);
  const int bid = blockIdx.x;
  const int m = ((bid & 7) & 1) * 8 + ((bid >> 3) >> 2), j = ((bid & 7) >> 1) * 4 + ((bid >> 3) & 3);
  const char* wsrc[4];
#pragma unroll
  for (int i = 0; i < 4; ++i) {
    const int q = (w < 4) ? (4 * w + i) : (16 + 3 * (w - 4) + (i < 3 ? i : 0));
    const int off = q * 1024 + lane * 16;
    const int row = off / 448, rem = off % 448;
    const bool up = rem >= 224;
    wsrc[i] = (const char*)(up ? wu : wg) + (size_t)row * (F_ * 2) + j * 224 + (up ? rem - 224 : rem);
  }
  const char* hsrc = ws + WS_HB + ((size_t)(m * 4 + (w >> 1)) * 28 + 2 * (w & 1)) * 1024 + lane * 16;
  if (w < 4) p4_body<0, 4>(lds, wsrc, hsrc, w, lane, m, j, ws);
  else p4_body<4, 3>(lds, wsrc, hsrc, w, lane, m, j, ws);
}

__global__ __launch_bounds__(512, 2) void k_p5(const __bf16* __restrict__ wd, const char* __restrict__ ws, __bf16* __restrict__ out) {
  extern __shared__ __attribute__((aligned(16))) char smem_[];
  LAS char* lds = (LAS char*)smem_;
  const int tid = threadIdx.x, lane = tid & 63;
  const int w = __builtin_amdgcn_readfirstlane(tid >> 6);
  const int bid = blockIdx.x;
  const int mp = (bid & 7) * 4 + ((bid >> 3) & 3), jn = bid >> 5;
  const int kq = w & 3, tt = w >> 2;
  const int t32 = lane & 31, hh = lane >> 5, blk = (lane >> 4) & 1, q4 = (lane & 15) >> 2, p4 = lane & 3;
  const int ttg = mp * 2 + tt;
  const char* wsrc[7];
#pragma unroll
  for (int i = 0; i < 7; ++i) {
    const int row = 8 * (tt * 7 + i) + (lane >> 3), c = (lane & 7) ^ wrow_swz(row);
    wsrc[i] = (const char*)wd + (size_t)(kq * 448 + row) * (D_ * 2) + jn * 128 + c * 16;
  }
  auto issue = [&](int c) {
    LAS char* slot = lds + (kq * 2 + (c & 1)) * 14336;
#pragma unroll
    for (int i = 0; i < 7; ++i) glds16(wsrc[i] + (size_t)c * 112 * (D_ * 2), slot + (tt * 7 + i) * 1024);
  };
  const bf16x8* hsrc = (const bf16x8*)(ws + WS_HH + ((size_t)ttg * 112 + kq * 28) * 1024) + lane;
  bf16x8 hb[28];
#pragma unroll
  for (int i = 0; i < 28; ++i) hb[i] = hsrc[i * 64];
  const u32x4 res = *((const u32x4*)(ws + WS_HB + ((size_t)ttg * 28 + jn * 4 + kq) * 1024) + lane);
  issue(0); issue(1);
  f32x16 a0 = {}, a1 = {};
  const int rowl = 4 * hh + q4;
  const int sw = wrow_swz(rowl);
  const int c0 = 2 * blk + (p4 >> 1);
  const int o0 = rowl * 128 + 8 * (p4 & 1) + ((c0 ^ sw) * 16), o1 = rowl * 128 + 8 * (p4 & 1) + (((c0 + 4) ^ sw) * 16);
#pragma unroll
  for (int c = 0; c < 4; ++c) {
    if (c < 3) WAIT_V(7); else WAIT_V(0);
    WAIT_L0;
    BAR;
    LAS char* slot = lds + (kq * 2 + (c & 1)) * 14336;
#pragma unroll
    for (int s = 0; s < 7; ++s) {
      LAS char* p = slot + s * 16 * 128;
      bf16x8 w0 = tr_frag(p + o0, p + o0 + 8 * 128);
      bf16x8 w1 = tr_frag(p + o1, p + o1 + 8 * 128);
      a0 = MFMA(w0, hb[c * 7 + s], a0);
      a1 = MFMA(w1, hb[c * 7 + s], a1);
    }
    if (c + 2 < 4) { WAIT_L0; BAR; issue(c + 2); }
  }
  WAIT_L0;
  BAR;
  float o[8];
  reduce4(lds, a0, a1, kq, tt, lane, o);
  o[0] += bf_lo(res[0]); o[1] += bf_hi(res[0]); o[2] += bf_lo(res[1]); o[3] += bf_hi(res[1]);
  o[4] += bf_lo(res[2]); o[5] += bf_hi(res[2]); o[6] += bf_lo(res[3]); o[7] += bf_hi(res[3]);
  unsigned a_x = pk2(o[0], o[1]), a_y = pk2(o[2], o[3]), b_x = pk2(o[4], o[5]), b_y = pk2(o[6], o[7]);
  auto r0 = __builtin_amdgcn_permlane32_swap(a_x, b_x, false, false);
  auto r1 = __builtin_amdgcn_permlane32_swap(a_y, b_y, false, false);
  u32x4 ov = {r0[0], r1[0], r0[1], r1[1]};
  *(u32x4*)((char*)out + (size_t)(ttg * 32 + t32) * (D_ * 2) + (jn * 64 + kq * 16 + 8 * hh) * 2) = ov;
}

extern "C" void kernel_launch(void* const* d_in, const int* in_sizes, int n_in, void* d_out, int out_size, void* d_ws,
                              size_t ws_size, hipStream_t stream) {
  const __bf16* x = (const __bf16*)d_in[0];
  const __bf16* wqkv = (const __bf16*)d_in[1];
  const __bf16* wphi = (const __bf16*)d_in[2];
  const __bf16* wo = (const __bf16*)d_in[3];
  const __bf16* wg = (const __bf16*)d_in[5];
  const __bf16* wu = (const __bf16*)d_in[6];
  const __bf16* wd = (const __bf16*)d_in[7];
  char* ws = (char*)d_ws;
  (void)hipFuncSetAttribute((const void*)k_p1, hipFuncAttributeMaxDynamicSharedMemorySize, P1_LDS);
  (void)hipFuncSetAttribute((const void*)k_p3, hipFuncAttributeMaxDynamicSharedMemorySize, 57344 + 65536);
  (void)hipFuncSetAttribute((const void*)k_p4, hipFuncAttributeMaxDynamicSharedMemorySize, P4_LDS);
  (void)hipFuncSetAttribute((const void*)k_p5, hipFuncAttributeMaxDynamicSharedMemorySize, 114688);
  k_p1<<<256, 512, P1_LDS, stream>>>(x, wqkv, wphi, ws);
  k_p2<<<256, 512, 4096, stream>>>(ws);
  k_p3<<<224, 512, 57344 + 65536, stream>>>(x, wo, ws);
  k_p4<<<256, 512, P4_LDS, stream>>>(wg, wu, ws);
  k_p5<<<224, 512, 114688, stream>>>(wd, ws, (__bf16*)d_out);
}
```

```cpp
#include <hip/hip_runtime.h>
#include <hip/hip_bf16.h>
#include <stdint.h>

typedef __attribute__((ext_vector_type(8))) __bf16 bf16x8;
typedef __attribute__((ext_vector_type(2))) __bf16 bf16x2;
typedef __attribute__((ext_vector_type(4))) short s16x4;
typedef __attribute__((ext_vector_type(8))) short s16x8;
typedef __attribute__((ext_vector_type(16))) float f32x16;
typedef __attribute__((ext_vector_type(4))) float f32x4;
typedef __attribute__((ext_vector_type(2))) float f32x2;
typedef __attribute__((ext_vector_type(4))) unsigned u32x4;
typedef __attribute__((ext_vector_type(2))) unsigned u32x2;
typedef unsigned long long u64;
#define LAS __attribute__((address_space(3)))
#define GAS __attribute__((address_space(1)))
#define DI __device__ __forceinline__
#define MFMA(a, b, c) __builtin_amdgcn_mfma_f32_32x32x16_bf16((a), (b), (c), 0, 0, 0)
#define WAIT_V(n) asm volatile("s_waitcnt vmcnt(" #n ")" ::: "memory")
#define WAIT_L0 asm volatile("s_waitcnt lgkmcnt(0)" ::: "memory")
#define FENCE() do { asm volatile("" ::: "memory"); __builtin_amdgcn_sched_barrier(0); } while (0)
#define BARRIER() do { __builtin_amdgcn_sched_barrier(0); asm volatile("s_waitcnt lgkmcnt(0)" ::: "memory"); __builtin_amdgcn_s_barrier(); FENCE(); } while (0)
#define RLX_AGENT __ATOMIC_RELAXED, __HIP_MEMORY_SCOPE_AGENT

constexpr int D_ = 448, F_ = 1792;
constexpr float LOG2E = 1.4426950408889634f;

constexpr unsigned WS_E = 0;
constexpr unsigned WS_F1 = 4096;
constexpr unsigned WS_F2 = WS_F1 + 4096;
constexpr unsigned WS_F3 = WS_F2 + 8192;
constexpr unsigned WS_F4 = WS_F3 + 16384;
constexpr unsigned WS_KVP = 65536;
constexpr unsigned WS_O = WS_KVP + (2u << 20);
constexpr unsigned WS_HB = WS_O + (2u << 20);
constexpr unsigned WS_HH = WS_HB + (2u << 20);
constexpr unsigned WS_DBG = WS_HH + (8u << 20);
constexpr unsigned WS_BYTES = WS_DBG + (1u << 20);

constexpr int SLOT = 28672;
constexpr int TAIL = 5 * SLOT;
constexpr int L_STAT = TAIL;
constexpr int L_CS1 = TAIL + 1024;
constexpr int L_KVA = TAIL + 4096;
constexpr int L_CS4 = TAIL + 8192;
constexpr int L_WPH = TAIL + 16384;
constexpr int LDS_BYTES = 163840;
constexpr int T_PITCH = 200;

DI void glds_s(unsigned voff, const void* sbase, unsigned lds_dst) {
  asm volatile("s_mov_b32 m0, %2\n\ts_nop 0\n\tglobal_load_lds_dwordx4 %0, %1" ::"v"(voff), "s"(sbase), "s"(lds_dst) : "memory");
}
DI void glds_v(const void* vaddr, unsigned lds_dst) {
  asm volatile("s_mov_b32 m0, %1\n\ts_nop 0\n\tglobal_load_lds_dwordx4 %0, off" ::"v"(vaddr), "s"(lds_dst) : "memory");
}
DI bf16x8 tr_frag(LAS char* p0, LAS char* p1) {
  s16x4 a = __builtin_amdgcn_ds_read_tr16_b64_v4i16((LAS s16x4*)p0);
  s16x4 b = __builtin_amdgcn_ds_read_tr16_b64_v4i16((LAS s16x4*)p1);
  s16x8 r = __builtin_shufflevector(a, b, 0, 1, 2, 3, 4, 5, 6, 7);
  return __builtin_bit_cast(bf16x8, r);
}
DI unsigned pk2(float a, float b) {
  f32x2 v = {a, b};
  bf16x2 r = __builtin_convertvector(v, bf16x2);
  return __builtin_bit_cast(unsigned, r);
}
template <int S>
DI u32x4 pack8(const f32x16& x) {
  u32x4 p;
  p[0] = pk2(x[8 * S + 0], x[8 * S + 1]);
  p[1] = pk2(x[8 * S + 2], x[8 * S + 3]);
  p[2] = pk2(x[8 * S + 4], x[8 * S + 5]);
  p[3] = pk2(x[8 * S + 6], x[8 * S + 7]);
  return p;
}
DI bf16x8 asbf(const u32x4& u) { return __builtin_bit_cast(bf16x8, u); }
DI float dot2(unsigned a, unsigned b, float c) {
  return __builtin_amdgcn_fdot2_f32_bf16(__builtin_bit_cast(bf16x2, a), __builtin_bit_cast(bf16x2, b), c, false);
}
constexpr unsigned ONES2 = 0x3F803F80u;
DI float frag_sum(const bf16x8& f, float c) {
  u32x4 u = __builtin_bit_cast(u32x4, f);
  c = dot2(u[0], ONES2, c); c = dot2(u[1], ONES2, c); c = dot2(u[2], ONES2, c); c = dot2(u[3], ONES2, c);
  return c;
}
DI float frag_sumsq(const bf16x8& f, float c) {
  u32x4 u = __builtin_bit_cast(u32x4, f);
  c = dot2(u[0], u[0], c); c = dot2(u[1], u[1], c); c = dot2(u[2], u[2], c); c = dot2(u[3], u[3], c);
  return c;
}
DI void xhalf(float x, float& lo, float& hi) {
  unsigned u = __builtin_bit_cast(unsigned, x);
  auto r = __builtin_amdgcn_permlane32_swap(u, u, false, false);
  lo = __builtin_bit_cast(float, (unsigned)r[0]);
  hi = __builtin_bit_cast(float, (unsigned)r[1]);
}
DI float xsum(float x) { float a, b; xhalf(x, a, b); return a + b; }
DI float xmax(float x) { float a, b; xhalf(x, a, b); return fmaxf(a, b); }
DI float xmin(float x) { float a, b; xhalf(x, a, b); return fminf(a, b); }
DI float bf_lo(unsigned u) { return __builtin_bit_cast(float, u << 16); }
DI float bf_hi(unsigned u) { return __builtin_bit_cast(float, u & 0xffff0000u); }

DI void dual_softmax(const f32x16& p, int hh, f32x16& ep, f32x16& en) {
  float mx = p[0], mn = p[0];
#pragma unroll
  for (int r = 1; r < 12; ++r) { mx = fmaxf(mx, p[r]); mn = fminf(mn, p[r]); }
  float mx2 = mx, mn2 = mn;
#pragma unroll
  for (int r = 12; r < 16; ++r) { mx2 = fmaxf(mx2, p[r]); mn2 = fminf(mn2, p[r]); }
  mx = hh ? mx : mx2; mn = hh ? mn : mn2;
  mx = xmax(mx); mn = xmin(mn);
  const float a = -mx * LOG2E, b = mn * LOG2E;
  float sp = 0.f, sn = 0.f;
#pragma unroll
  for (int r = 0; r < 16; ++r) {
    float e1 = __builtin_amdgcn_exp2f(fmaf(p[r], LOG2E, a));
    float e2 = __builtin_amdgcn_exp2f(fmaf(p[r], -LOG2E, b));
    if (r >= 12) { e1 = hh ? 0.f : e1; e2 = hh ? 0.f : e2; }
    ep[r] = e1; en[r] = e2; sp += e1; sn += e2;
  }
  sp = xsum(sp); sn = xsum(sn);
  const float rp = __builtin_amdgcn_rcpf(sp), rn = __builtin_amdgcn_rcpf(sn);
#pragma unroll
  for (int r = 0; r < 16; ++r) { ep[r] *= rp; en[r] *= rn; }
}

DI u32x4 ld_sc1(__amdgpu_buffer_rsrc_t rs, unsigned voff, unsigned soff) {
  return __builtin_amdgcn_raw_buffer_load_b128(rs, voff, soff, 16);
}
DI void st_sc1(__amdgpu_buffer_rsrc_t rs, const u32x4& v, unsigned voff, unsigned soff) {
  __builtin_amdgcn_raw_buffer_store_b128(v, rs, voff, soff, 16);
}
DI void wait_flags(char* ws, unsigned off, bool active, u64 tag) {
  GAS u64* p = (GAS u64*)(ws + off);
  GAS u64* ab = (GAS u64*)(ws + WS_E + 128);
  for (unsigned spins = 0;; ++spins) {
    u64 v = tag;
    if (active) v = __hip_atomic_load(p, RLX_AGENT);
    if (__all(v == tag)) break;
    __builtin_amdgcn_s_sleep(1);
    if ((spins & 63u) == 63u) {
      if (spins > 20000u) { __hip_atomic_store(ab, tag, RLX_AGENT); break; }
      if (__hip_atomic_load(ab, RLX_AGENT) == tag) break;
    }
  }
  asm volatile("" ::: "memory");
}
DI void set_flag(char* ws, unsigned off, u64 tag, int lane) {
  asm volatile("s_waitcnt vmcnt(0)" ::: "memory");
  if (lane == 0) __hip_atomic_store((GAS u64*)(ws + off), tag, RLX_AGENT);
}

#define STAMP(i) do {} while (0)

DI int wrow_swz(int row) { return ((row >> 1) & 1) << 2; }

template <int KQ>
DI void reduce_write_t(LAS char* red, const f32x16 (&a)[2][2], int lane, float (&o)[2][8]) {
#pragma unroll
  for (int tt = 0; tt < 2; ++tt)
#pragma unroll
    for (int p = 0; p < 4; ++p) {
      const f32x16& v = a[tt][p >> 1];
      const int rb = (p & 1) * 8;
      if (p != KQ) {
        f32x4 v0 = {v[rb], v[rb + 1], v[rb + 2], v[rb + 3]}, v1 = {v[rb + 4], v[rb + 5], v[rb + 6], v[rb + 7]};
        LAS char* d = red + (((tt * 4 + p) * 3 + ((KQ - p - 1) & 3)) * 2) * 1024 + lane * 16;
        *(LAS f32x4*)d = v0; *(LAS f32x4*)(d + 1024) = v1;
      } else {
#pragma unroll
        for (int i = 0; i < 8; ++i) o[tt][i] = v[rb + i];
      }
    }
}
DI void reduce_write(LAS char* red, const f32x16 (&a)[2][2], int kq, int lane, float (&o)[2][8]) {
  if (kq < 2) { if (kq == 0) reduce_write_t<0>(red, a, lane, o); else reduce_write_t<1>(red, a, lane, o); }
  else { if (kq == 2) reduce_write_t<2>(red, a, lane, o); else reduce_write_t<3>(red, a, lane, o); }
}
DI void reduce_read(LAS char* red, int kq, int lane, float (&o)[2][8]) {
#pragma unroll
  for (int tt = 0; tt < 2; ++tt)
#pragma unroll
    for (int sq = 0; sq < 3; ++sq) {
      LAS char* d = red + (((tt * 4 + kq) * 3 + sq) * 2) * 1024 + lane * 16;
      f32x4 v0 = *(LAS f32x4*)d, v1 = *(LAS f32x4*)(d + 1024);
#pragma unroll
      for (int i = 0; i < 4; ++i) { o[tt][i] += v0[i]; o[tt][4 + i] += v1[i]; }
    }
}

__host__ __device__ constexpr int p5_ftile(int i) { return i < 16 ? 7 * (i >> 2) + (i & 3) : 7 * ((i - 16) / 3) + 4 + (i - 16) % 3; }
__host__ __device__ constexpr int d_slot(int n) { return n == 0 ? 2 : n == 1 ? 3 : n == 2 ? 0 : n == 3 ? 1 : n == 4 ? 4 : n == 5 ? 2 : n == 6 ? 3 : 0; }
__host__ __device__ constexpr int a_slot(int n) { return n == 0 ? 0 : n == 1 ? 1 : n == 2 ? 4 : 2; }
__host__ __device__ constexpr int b_slot(int n) { return n == 0 ? 3 : n == 1 ? 0 : n == 2 ? 1 : 4; }

__global__ __launch_bounds__(512, 2) void fused_block(const __bf16* __restrict__ x, const __bf16* __restrict__ wqkv,
                                                      const __bf16* __restrict__ wphi, const __bf16* __restrict__ wo,
                                                      const __bf16* __restrict__ wg, const __bf16* __restrict__ wu,
                                                      const __bf16* __restrict__ wd, char* ws, __bf16* __restrict__ out) {
  extern __shared__ __attribute__((aligned(16))) char smem_[];
  LAS char* lds = (LAS char*)smem_;
  const unsigned lbase = (unsigned)(size_t)lds;
  const int tid = threadIdx.x, lane = tid & 63;
  const int w = __builtin_amdgcn_readfirstlane(tid >> 6);
  const int bid = blockIdx.x, g8 = bid & 7, r32 = bid >> 3;
  const int wi = w & 3;
  const bool hi4 = w >= 4;
  const int t32 = lane & 31, hh = lane >> 5, blk = (lane >> 4) & 1, q4 = (lane & 15) >> 2, p4 = lane & 3;
  const int m1 = (g8 & 3) * 4 + (r32 >> 3), h = (g8 >> 2) * 8 + (r32 & 7), hodd = h & 1;
  const int mp = g8 * 4 + (r32 & 3), jn = r32 >> 2;
  const bool act35 = jn < 7;
  const int m4 = g8 * 2 + (r32 >> 4), j4 = r32 & 15;
  const __amdgpu_buffer_rsrc_t rs = __builtin_amdgcn_make_buffer_rsrc((void*)ws, 0, WS_BYTES, 0x00020000);
  STAMP(0);

  const u64 tag = *(const __attribute__((address_space(4))) u64*)(ws + WS_E) + 1;

  unsigned xv[2], wv[2];
#pragma unroll
  for (int i = 0; i < 2; ++i) {
    const int row = 8 * (2 * w + i) + (lane >> 3);
    const int c = (lane & 7) ^ ((row >> 1) & 7);
    xv[i] = row * (D_ * 2) + c * 16;
    const int q = hi4 ? (8 + wi) : (2 * w + i);
    const int off = q * 1024 + lane * 16;
    const int wr = off / 192, rem = off % 192, seg = rem >> 6, cc = (rem & 63) >> 4;
    wv[i] = wr * (3 * D_ * 2) + seg * (D_ * 2) + cc * 16;
  }
  const char* xb_ = (const char*)x + (size_t)m1 * 128 * (D_ * 2);
  const char* wb_ = (const char*)wqkv + (56 * h - 8 * hodd);
  auto p1_issue = [&](int c) {
    const unsigned slot = lbase + (c % 5) * SLOT;
    glds_s(xv[0], xb_ + c * 128, slot + (2 * w) * 1024);
    glds_s(xv[1], xb_ + c * 128, slot + (2 * w + 1) * 1024);
    if (hi4) {
      glds_s(wv[0], wb_ + (size_t)c * 64 * (3 * D_ * 2), slot + 16384 + (8 + wi) * 1024);
    } else {
      glds_s(wv[0], wb_ + (size_t)c * 64 * (3 * D_ * 2), slot + 16384 + (2 * w) * 1024);
      glds_s(wv[1], wb_ + (size_t)c * 64 * (3 * D_ * 2), slot + 16384 + (2 * w + 1) * 1024);
    }
  };
  u32x2 wphv = {0u, 0u};
  bool wph_ok = false;
  if (hi4) {
#pragma unroll
    for (int c = 0; c < 5; ++c) p1_issue(c);
    const int idx = wi * 64 + lane, d = (idx >> 3) - 4 * hodd, u = idx & 7;
    wph_ok = (d >= 0) && (d < 28) && (u < 7);
    const unsigned off = wph_ok ? (unsigned)((h * 28 + d) * 56 + u * 8) : 0u;
    asm volatile("global_load_dwordx2 %0, %1, %2" : "=v"(wphv) : "v"(off), "s"(wphi) : "memory");
  } else {
#pragma unroll
    for (int c = 0; c < 5; ++c) p1_issue(c);
  }
  STAMP(29);
  const unsigned v128 = (lane >> 3) * (D_ * 2) + (((lane & 7) ^ wrow_swz(lane >> 3)) * 16) + (act35 ? jn : 0) * 128;
  auto wo_issue = [&](int half) {
#pragma unroll
    for (int i = 0; i < 7; ++i) {
      const int g = half * 28 + wi * 7 + i;
      glds_s(v128, (const char*)wo + (size_t)g * 8 * (D_ * 2), lbase + 2 * SLOT + g * 1024);
    }
  };
  const char* av;
  {
    const int row = lane >> 4, c = (lane & 15) ^ (2 * row);
    av = (const char*)(c < 8 ? wg : wu) + (size_t)(wi * 28 + row) * (F_ * 2) + j4 * 224 + (c & 7) * 16;
  }
  auto a_issue = [&](int n) {
#pragma unroll
    for (int i = 0; i < 7; ++i)
      glds_v(av + (size_t)(n * 112 + 4 * i) * (F_ * 2), lbase + a_slot(n) * SLOT + (wi * 7 + i) * 1024);
  };

  f32x16 acc0 = {}, acc1 = {};
  float ssum = 0.f, ssq = 0.f, cs0 = 0.f, cs1 = 0.f;
  const int xrow = wi * 32 + t32;
  {
    int xo[4], wo_[4];
#pragma unroll
    for (int i = 0; i < 4; ++i) {
      const int s = (wi + i) & 3;
      xo[i] = xrow * 128 + (((2 * s + hh) ^ ((xrow >> 1) & 7)) * 16);
      wo_[i] = 16384 + (16 * s + 8 * hh + q4) * 192 + blk * 32 + p4 * 8 + (hi4 ? 0 : 64);
    }
    if (hi4) {
#pragma unroll
      for (int c = 0; c < 7; ++c) {
        if (c == 0) WAIT_V(13);
        else if (c <= 3) WAIT_V(10);
        else if (c == 4) WAIT_V(14);
        else if (c == 5) {
          asm volatile("s_waitcnt vmcnt(17)" : "+v"(wphv)::"memory");
          *(LAS u32x2*)(lds + L_WPH + (wi * 64 + lane) * 8) = wph_ok ? wphv : (u32x2){0u, 0u};
        } else WAIT_V(14);
        BARRIER();
        STAMP(17 + c);
        if (c == 1) p1_issue(5);
        if (c == 2) p1_issue(6);
        if (c == 3) wo_issue(0);
        if (c == 4) wo_issue(1);
        LAS char* slot = lds + (c % 5) * SLOT;
#pragma unroll
        for (int i = 0; i < 4; ++i) {
          bf16x8 xb = *(LAS bf16x8*)(slot + xo[i]);
          bf16x8 a0 = tr_frag(slot + wo_[i], slot + wo_[i] + 4 * 192);
          acc0 = MFMA(a0, xb, acc0);
          if (i == 0) cs0 = frag_sum(a0, cs0);
          ssum = frag_sum(xb, ssum);
          ssq = frag_sumsq(xb, ssq);
        }
      }
    } else {
#pragma unroll
      for (int c = 0; c < 7; ++c) {
        if (c == 0) WAIT_V(16);
        else if (c <= 3) WAIT_V(12); else if (c == 4) WAIT_V(8); else if (c == 5) WAIT_V(4); else WAIT_V(0);
        BARRIER();
        STAMP(17 + c);
        if (c == 1) p1_issue(5);
        if (c == 2) p1_issue(6);
        LAS char* slot = lds + (c % 5) * SLOT;
#pragma unroll
        for (int i = 0; i < 4; ++i) {
          bf16x8 xb = *(LAS bf16x8*)(slot + xo[i]);
          bf16x8 a0 = tr_frag(slot + wo_[i], slot + wo_[i] + 4 * 192);
          bf16x8 a1 = tr_frag(slot + wo_[i] + 64, slot + wo_[i] + 64 + 4 * 192);
          acc0 = MFMA(a0, xb, acc0);
          acc1 = MFMA(a1, xb, acc1);
          if (i == 0) { cs0 = frag_sum(a0, cs0); cs1 = frag_sum(a1, cs1); }
        }
      }
    }
  }
  STAMP(1);
  {
    cs0 = xsum(cs0); cs1 = xsum(cs1);
    LAS float* csp = (LAS float*)(lds + L_CS1);
    LAS float* stat = (LAS float*)(lds + L_STAT);
    if (hi4) {
      ssum = xsum(ssum); ssq = xsum(ssq);
      const float mu = ssum * (1.0f / D_);
      const float var = fmaxf(ssq * (1.0f / D_) - mu * mu, 0.f);
      const float rstd = __builtin_amdgcn_rsqf(var + 1e-5f);
      if (hh == 0) { stat[2 * xrow] = mu; stat[2 * xrow + 1] = rstd; csp[wi * 96 + t32] = cs0; }
    } else if (hh == 0) {
      csp[wi * 96 + 32 + t32] = cs0;
      csp[wi * 96 + 64 + t32] = cs1;
    }
    BARRIER();
    STAMP(24);
    if (hi4) { a_issue(0); a_issue(1); }
    STAMP(25);
    const float mu = stat[2 * xrow], rstd = stat[2 * xrow + 1];
    const int seg0 = hi4 ? 0 : 32;
#pragma unroll
    for (int g = 0; g < 4; ++g) {
      f32x4 c0 = {}, c1 = {};
#pragma unroll
      for (int i = 0; i < 4; ++i) {
        c0 += *(LAS f32x4*)(csp + i * 96 + seg0 + 8 * g + 4 * hh);
        c1 += *(LAS f32x4*)(csp + i * 96 + 64 + 8 * g + 4 * hh);
      }
#pragma unroll
      for (int r = 0; r < 4; ++r) {
        acc0[4 * g + r] = rstd * (acc0[4 * g + r] - mu * c0[r]);
        acc1[4 * g + r] = rstd * (acc1[4 * g + r] - mu * c1[r]);
      }
    }
  }
  u32x4 phq[4];
  {
    f32x16 pq = {};
    LAS char* wp = lds + L_WPH + (4 * hh + q4) * 64 + blk * 32 + p4 * 8;
    pq = MFMA(tr_frag(wp, wp + 512), asbf(pack8<0>(acc0)), pq);
    pq = MFMA(tr_frag(wp + 1024, wp + 1536), asbf(pack8<1>(acc0)), pq);
    f32x16 ep, en;
    dual_softmax(pq, hh, ep, en);
    phq[0] = pack8<0>(ep); phq[1] = pack8<1>(ep); phq[2] = pack8<0>(en); phq[3] = pack8<1>(en);
    LAS char* T = lds + 4 * SLOT;
    if (!hi4) {
      if (hodd) { if (hh == 0) acc1[0] = 1.0f; } else { if (hh == 1) acc1[12] = 1.0f; }
      const u32x4 v0 = pack8<0>(acc1), v1 = pack8<1>(acc1);
      LAS char* tp = T + xrow * T_PITCH + 8 * hh;
#pragma unroll
      for (int g = 0; g < 4; ++g) {
        const u32x4& a = phq[g >> 1]; const u32x4& b = phq[2 + (g >> 1)]; const u32x4& c = (g >> 1) ? v1 : v0;
        const int e = (g & 1) * 2;
        *(LAS u32x2*)(tp + 16 * g) = (u32x2){a[e], a[e + 1]};
        *(LAS u32x2*)(tp + 64 + 16 * g) = (u32x2){b[e], b[e + 1]};
        *(LAS u32x2*)(tp + 128 + 16 * g) = (u32x2){c[e], c[e + 1]};
      }
    }
    STAMP(26);
    BARRIER();
    STAMP(27);
    if (w < 2) {
      f32x16 kv = {};
      LAS char* ap = T + (8 * hh + q4) * T_PITCH + blk * 32 + p4 * 8;
#pragma unroll
      for (int s = 0; s < 8; ++s) {
        LAS char* p = ap + s * 16 * T_PITCH;
        bf16x8 a = tr_frag(p + w * 64, p + w * 64 + 4 * T_PITCH);
        bf16x8 b = tr_frag(p + 128, p + 128 + 4 * T_PITCH);
        kv = MFMA(a, b, kv);
      }
      const unsigned so = WS_KVP + ((h * 16 + m1) * 8 + w * 4) * 1024;
#pragma unroll
      for (int g = 0; g < 4; ++g) {
        f32x4 v = {kv[4 * g], kv[4 * g + 1], kv[4 * g + 2], kv[4 * g + 3]};
        st_sc1(rs, __builtin_bit_cast(u32x4, v), lane * 16 + g * 1024, so);
      }
      STAMP(28);
      set_flag(ws, WS_F1 + ((h * 2 + w) * 16 + m1) * 8, tag, lane);
    }
  }
  STAMP(2);
  {
    wait_flags(ws, WS_F1 + ((h * 2 + (w >> 2)) * 16 + (lane & 15)) * 8, lane < 16, tag);
    STAMP(3);
    f32x4 s = {};
    u32x4 v[16];
#pragma unroll
    for (int i = 0; i < 16; ++i) v[i] = ld_sc1(rs, lane * 16, WS_KVP + ((h * 16 + i) * 8 + w) * 1024);
#pragma unroll
    for (int i = 0; i < 16; ++i) s += __builtin_bit_cast(f32x4, v[i]);
    *(LAS u32x2*)(lds + L_KVA + (w >> 1) * 1024 + lane * 16 + (w & 1) * 8) = (u32x2){pk2(s[0], s[1]), pk2(s[2], s[3])};
    BARRIER();
    STAMP(4);
    if (hi4) {
      f32x16 num = {};
#pragma unroll
      for (int i = 0; i < 4; ++i) num = MFMA(*(LAS bf16x8*)(lds + L_KVA + i * 1024 + lane * 16), asbf(phq[i]), num);
      float lo, hi;
      xhalf(hodd ? num[0] : num[12], lo, hi);
      const float rd = 1.0f / ((hodd ? lo : hi) + 1e-6f);
#pragma unroll
      for (int r = 0; r < 16; ++r) num[r] *= rd;
      if (hodd) { if (hh == 0) { num[0] = 0.f; num[1] = 0.f; num[2] = 0.f; num[3] = 0.f; } }
      else { if (hh == 1) { num[12] = 0.f; num[13] = 0.f; num[14] = 0.f; num[15] = 0.f; } }
      const unsigned so = WS_O + (((m1 * 4 + wi) * 16 + h) * 2) * 1024;
      st_sc1(rs, pack8<0>(num), lane * 16, so);
      st_sc1(rs, pack8<1>(num), lane * 16 + 1024, so);
      set_flag(ws, WS_F2 + ((m1 * 4 + wi) * 16 + h) * 8, tag, lane);
    } else {
      a_issue(2);
    }
  }
  STAMP(5);
  const char* bv[3];
#pragma unroll
  for (int p = 0; p < 3; ++p) {
    const int off = p * 1024 + lane * 16, row = off / 192, rem = off % 192;
    bv[p] = (const char*)(rem < 96 ? wg : wu) + (size_t)row * (F_ * 2) + j4 * 224 + 128 + (rem < 96 ? rem : rem - 96);
  }
  auto b_issue = [&](int n) {
    if (n < 3) {
#pragma unroll
      for (int g = 0; g < 2; ++g)
#pragma unroll
        for (int p = 0; p < 3; ++p)
          glds_v(bv[p] + (size_t)(n * 128 + wi * 32 + g * 16) * (F_ * 2), lbase + b_slot(n) * SLOT + ((wi * 2 + g) * 3 + p) * 1024);
    } else {
#pragma unroll
      for (int p = 0; p < 3; ++p)
        glds_v(bv[p] + (size_t)(384 + wi * 16) * (F_ * 2), lbase + b_slot(3) * SLOT + (wi * 3 + p) * 1024);
    }
  };
  auto d_issue = [&](int n) {
    if (act35) {
#pragma unroll
      for (int i = 0; i < 7; ++i) {
        const int g = n * 7 + i;
        const int row = 16 * p5_ftile(g >> 1) + 8 * (g & 1);
        glds_s(v128, (const char*)wd + (size_t)(wi * 448 + row) * (D_ * 2), lbase + d_slot(n) * SLOT + (wi * 7 + i) * 1024);
      }
    }
  };
  const int o128a = (4 * hh + q4) * 128 + 8 * (p4 & 1) + (((2 * blk + (p4 >> 1)) ^ wrow_swz(q4)) * 16);
  const int o128b = (4 * hh + q4) * 128 + 8 * (p4 & 1) + (((4 + 2 * blk + (p4 >> 1)) ^ wrow_swz(q4)) * 16);

  if (act35) {
    if (!hi4) {
      const int kq = wi;
      u32x2 xr[2][2];
#pragma unroll
      for (int tt = 0; tt < 2; ++tt) {
        const char* p = (const char*)x + (size_t)((mp * 2 + tt) * 32 + t32) * (D_ * 2) + (jn * 64 + kq * 16 + 4 * hh) * 2;
        xr[tt][0] = *(const u32x2*)p; xr[tt][1] = *(const u32x2*)(p + 16);
      }
      wait_flags(ws, WS_F2 + ((mp * 2 + ((lane >> 2) & 1)) * 16 + kq * 4 + (lane & 3)) * 8, lane < 8, tag);
      STAMP(6);
      u32x4 ob[2][8];
#pragma unroll
      for (int i = 0; i < 8; ++i)
#pragma unroll
        for (int tt = 0; tt < 2; ++tt) ob[tt][i] = ld_sc1(rs, lane * 16 + i * 1024, WS_O + (((mp * 2 + tt) * 16 + kq * 4) * 2) * 1024);
      FENCE();
      f32x16 a[2][2] = {};
      LAS char* wb = lds + 2 * SLOT + kq * 112 * 128;
#pragma unroll
      for (int i = 0; i < 8; ++i) {
        const int hd = i >> 1;
        LAS char* p = wb + (28 * hd - 4 * (hd & 1) + 16 * (i & 1)) * 128;
        bf16x8 w0 = tr_frag(p + o128a, p + o128a + 1024);
        bf16x8 w1 = tr_frag(p + o128b, p + o128b + 1024);
#pragma unroll
        for (int tt = 0; tt < 2; ++tt) {
          a[tt][0] = MFMA(w0, asbf(ob[tt][i]), a[tt][0]);
          a[tt][1] = MFMA(w1, asbf(ob[tt][i]), a[tt][1]);
        }
      }
      STAMP(7);
      BARRIER();
      float o[2][8];
      reduce_write(lds + 2 * SLOT, a, kq, lane, o);
      BARRIER();
      reduce_read(lds + 2 * SLOT, kq, lane, o);
      BARRIER();
#pragma unroll
      for (int tt = 0; tt < 2; ++tt) {
        u32x4 hv = {pk2(o[tt][0] + bf_lo(xr[tt][0][0]), o[tt][1] + bf_hi(xr[tt][0][0])), pk2(o[tt][2] + bf_lo(xr[tt][0][1]), o[tt][3] + bf_hi(xr[tt][0][1])),
                    pk2(o[tt][4] + bf_lo(xr[tt][1][0]), o[tt][5] + bf_hi(xr[tt][1][0])), pk2(o[tt][6] + bf_lo(xr[tt][1][1]), o[tt][7] + bf_hi(xr[tt][1][1]))};
        st_sc1(rs, hv, lane * 16, WS_HB + ((mp * 2 + tt) * 28 + jn * 4 + kq) * 1024);
      }
      asm volatile("s_waitcnt vmcnt(0)" ::: "memory");
      if (lane < 2) __hip_atomic_store((GAS u64*)(ws + WS_F3 + ((mp * 2 + lane) * 32 + jn * 4 + kq) * 8), tag, RLX_AGENT);
    } else {
      BARRIER(); BARRIER(); BARRIER();
    }
  }
  STAMP(8);
  if (hi4) {
    a_issue(3); b_issue(0);
    int ao = (4 * hh + q4) * 256 + 8 * (p4 & 1) + (((blk * 8 + 2 * wi + (p4 >> 1)) ^ (2 * q4)) * 16);
    float cs = 0.f;
    BARRIER();
#pragma unroll
    for (int n = 0; n < 4; ++n) {
      LAS char* sp = lds + a_slot(n) * SLOT + ao;
#pragma unroll
      for (int s = 0; s < 7; ++s) cs = frag_sum(tr_frag(sp + s * 4096, sp + s * 4096 + 2048), cs);
      if (n == 2) WAIT_V(18);
      if (n == 3) {
        cs = xsum(cs);
        if (hh == 0) *(LAS float*)(lds + L_CS4 + (wi * 32 + t32) * 4) = cs;
        WAIT_V(15);
      }
      BARRIER();
      if (n < 3) b_issue(n + 1); else d_issue(0);
    }
    const int wt = wi < 3 ? wi : 0;
    const int bo = (4 * hh + q4) * 192 + blk * 96 + wt * 32 + p4 * 8;
    cs = 0.f;
#pragma unroll
    for (int n = 0; n < 4; ++n) {
      LAS char* sp = lds + b_slot(n) * SLOT + bo;
#pragma unroll
      for (int s = 0; s < (n < 3 ? 8 : 4); ++s) cs = frag_sum(tr_frag(sp + s * 3072, sp + s * 3072 + 1536), cs);
      if (act35) {
        if (n == 0) WAIT_V(16);
        if (n == 1) WAIT_V(17);
        if (n == 2) WAIT_V(21);
      } else {
        if (n == 0) WAIT_V(9);
        if (n == 1) WAIT_V(3);
        if (n == 2) WAIT_V(0);
      }
      if (n == 3) {
        cs = xsum(cs);
        if (hh == 0 && wi < 3) *(LAS float*)(lds + L_CS4 + ((4 + wi) * 32 + t32) * 4) = cs;
      }
      BARRIER();
      d_issue(n + 1);
    }
    if (act35) {
      WAIT_V(21); BARRIER();
      BARRIER(); d_issue(5);
      WAIT_V(14); BARRIER(); d_issue(6);
      BARRIER(); d_issue(7);
      WAIT_V(14); BARRIER();
      WAIT_V(0); BARRIER();
      BARRIER(); BARRIER();
    }
  } else {
    const int ttg = m4 * 4 + wi;
    wait_flags(ws, WS_F3 + (ttg * 32 + (lane & 31)) * 8, lane < 28, tag);
    STAMP(9);
    u32x4 hb[28];
#pragma unroll
    for (int i = 0; i < 28; ++i) hb[i] = ld_sc1(rs, lane * 16 + (i & 3) * 1024, WS_HB + (ttg * 28 + (i & ~3)) * 1024);
    float ssum2 = 0.f, ssq2 = 0.f;
    float mu, rstd;
    const unsigned so = WS_HH + (ttg * 112 + j4 * 7) * 1024;
    {
      f32x16 acc[4] = {};
      int ao[4];
#pragma unroll
      for (int t = 0; t < 4; ++t) ao[t] = (4 * hh + q4) * 256 + 8 * (p4 & 1) + (((blk * 8 + 2 * t + (p4 >> 1)) ^ (2 * q4)) * 16);
      BARRIER();
#pragma unroll
      for (int n = 0; n < 4; ++n) {
        LAS char* sp = lds + a_slot(n) * SLOT;
#pragma unroll
        for (int s = 0; s < 7; ++s) {
          const bf16x8 b = asbf(hb[n * 7 + s]);
#pragma unroll
          for (int t = 0; t < 4; ++t) acc[t] = MFMA(tr_frag(sp + ao[t] + s * 4096, sp + ao[t] + s * 4096 + 2048), b, acc[t]);
          ssum2 = frag_sum(b, ssum2);
          ssq2 = frag_sumsq(b, ssq2);
        }
        BARRIER();
      }
      STAMP(10);
      ssum2 = xsum(ssum2); ssq2 = xsum(ssq2);
      mu = ssum2 * (1.0f / D_);
      rstd = __builtin_amdgcn_rsqf(fmaxf(ssq2 * (1.0f / D_) - mu * mu, 0.f) + 1e-5f);
#pragma unroll
      for (int t = 0; t < 4; ++t) {
        float v[16];
#pragma unroll
        for (int g = 0; g < 4; ++g) {
          const f32x4 c = *(LAS f32x4*)(lds + L_CS4 + (t * 32 + 8 * g + 4 * hh) * 4);
#pragma unroll
          for (int r = 0; r < 4; ++r) v[4 * g + r] = rstd * (acc[t][4 * g + r] - mu * c[r]);
        }
        float o[8];
#pragma unroll
        for (int r = 0; r < 8; ++r) o[r] = v[8 + r] * __builtin_amdgcn_rcpf(1.0f + __builtin_amdgcn_exp2f(-v[r] * LOG2E));
        st_sc1(rs, (u32x4){pk2(o[0], o[1]), pk2(o[2], o[3]), pk2(o[4], o[5]), pk2(o[6], o[7])}, lane * 16 + t * 1024, so);
      }
    }
    {
      f32x16 acc[3] = {};
      const int bo = (4 * hh + q4) * 192 + blk * 96 + p4 * 8;
#pragma unroll
      for (int n = 0; n < 4; ++n) {
        LAS char* sp = lds + b_slot(n) * SLOT + bo;
#pragma unroll
        for (int s = 0; s < (n < 3 ? 8 : 4); ++s) {
          const bf16x8 b = asbf(hb[n * 8 + s]);
#pragma unroll
          for (int t = 0; t < 3; ++t) acc[t] = MFMA(tr_frag(sp + t * 32 + s * 3072, sp + t * 32 + s * 3072 + 1536), b, acc[t]);
        }
        if (n == 0) set_flag(ws, WS_F4 + ((ttg * 16 + j4) * 2) * 8, tag, lane);
        BARRIER();
      }
      STAMP(11);
#pragma unroll
      for (int t = 0; t < 3; ++t) {
        float v[16];
#pragma unroll
        for (int g = 0; g < 4; ++g) {
          const f32x4 c = *(LAS f32x4*)(lds + L_CS4 + ((4 + t) * 32 + 8 * g + 4 * hh) * 4);
#pragma unroll
          for (int r = 0; r < 4; ++r) v[4 * g + r] = rstd * (acc[t][4 * g + r] - mu * c[r]);
        }
        float o[8];
#pragma unroll
        for (int r = 0; r < 8; ++r) o[r] = v[8 + r] * __builtin_amdgcn_rcpf(1.0f + __builtin_amdgcn_exp2f(-v[r] * LOG2E));
        st_sc1(rs, (u32x4){pk2(o[0], o[1]), pk2(o[2], o[3]), pk2(o[4], o[5]), pk2(o[6], o[7])}, lane * 16 + (t & 1) * 1024, so + (4 + (t & 2)) * 1024);
      }
      set_flag(ws, WS_F4 + ((ttg * 16 + j4) * 2 + 1) * 8, tag, lane);
    }
    STAMP(12);
    if (act35) {
      const int kq = wi;
      const unsigned f4 = WS_F4 + (((mp * 2 + ((lane >> 2) & 1)) * 16 + kq * 4 + (lane & 3)) * 2) * 8;
      wait_flags(ws, f4, lane < 8, tag);
      STAMP(13);
      u32x4 gb[28][2];
      auto hh_load = [&](int i) {
#pragma unroll
        for (int tt = 0; tt < 2; ++tt)
          gb[i][tt] = ld_sc1(rs, lane * 16, WS_HH + ((mp * 2 + tt) * 112 + kq * 28 + p5_ftile(i)) * 1024);
      };
#pragma unroll
      for (int i = 0; i < 16; ++i) hh_load(i);
      u32x4 res[2];
      f32x16 a[2][2] = {};
#pragma unroll
      for (int i = 0; i < 28; ++i) {
        if (i == 0 || i == 4 || i == 7 || i == 11 || i == 14 || i == 21) BARRIER();
        if (i == 9) {
          wait_flags(ws, f4 + 8, lane < 8, tag);
          STAMP(14);
#pragma unroll
          for (int k = 16; k < 28; ++k) hh_load(k);
          FENCE();
        }
        if (i == 21) {
#pragma unroll
          for (int tt = 0; tt < 2; ++tt) res[tt] = ld_sc1(rs, lane * 16, WS_HB + ((mp * 2 + tt) * 28 + jn * 4 + kq) * 1024);
        }
        const int g0 = 2 * i, g1 = 2 * i + 1;
        LAS char* p0 = lds + d_slot(g0 / 7) * SLOT + (kq * 7 + g0 % 7) * 1024;
        LAS char* p1 = lds + d_slot(g1 / 7) * SLOT + (kq * 7 + g1 % 7) * 1024;
        bf16x8 w0 = tr_frag(p0 + o128a, p1 + o128a);
        bf16x8 w1 = tr_frag(p0 + o128b, p1 + o128b);
#pragma unroll
        for (int tt = 0; tt < 2; ++tt) {
          a[tt][0] = MFMA(w0, asbf(gb[i][tt]), a[tt][0]);
          a[tt][1] = MFMA(w1, asbf(gb[i][tt]), a[tt][1]);
        }
      }
      STAMP(15);
      BARRIER();
      float o[2][8];
      reduce_write(lds, a, kq, lane, o);
      BARRIER();
      reduce_read(lds, kq, lane, o);
#pragma unroll
      for (int tt = 0; tt < 2; ++tt) {
        const u32x4 r = res[tt];
        unsigned a_x = pk2(o[tt][0] + bf_lo(r[0]), o[tt][1] + bf_hi(r[0])), a_y = pk2(o[tt][2] + bf_lo(r[1]), o[tt][3] + bf_hi(r[1]));
        unsigned b_x = pk2(o[tt][4] + bf_lo(r[2]), o[tt][5] + bf_hi(r[2])), b_y = pk2(o[tt][6] + bf_lo(r[3]), o[tt][7] + bf_hi(r[3]));
        auto r0 = __builtin_amdgcn_permlane32_swap(a_x, b_x, false, false);
        auto r1 = __builtin_amdgcn_permlane32_swap(a_y, b_y, false, false);
        u32x4 ov = {r0[0], r1[0], r0[1], r1[1]};
        *(u32x4*)((char*)out + (size_t)((mp * 2 + tt) * 32 + t32) * (D_ * 2) + (jn * 64 + kq * 16 + 8 * hh) * 2) = ov;
      }
      if (bid == 0 && tid == 0) __hip_atomic_store((GAS u64*)(ws + WS_E), tag, RLX_AGENT);
    }
    STAMP(16);
  }
}

extern "C" void kernel_launch(void* const* d_in, const int* in_sizes, int n_in, void* d_out, int out_size, void* d_ws,
                              size_t ws_size, hipStream_t stream) {
  (void)hipFuncSetAttribute((const void*)fused_block, hipFuncAttributeMaxDynamicSharedMemorySize, LDS_BYTES);
  fused_block<<<256, 512, LDS_BYTES, stream>>>((const __bf16*)d_in[0], (const __bf16*)d_in[1], (const __bf16*)d_in[2],
                                               (const __bf16*)d_in[3], (const __bf16*)d_in[5], (const __bf16*)d_in[6],
                                               (const __bf16*)d_in[7], (char*)d_ws, (__bf16*)d_out);
}
```

```cpp
#include <hip/hip_runtime.h>
#include <hip/hip_bf16.h>
#include <stdint.h>
#include <utility>

typedef __attribute__((ext_vector_type(8))) __bf16 bf16x8;
typedef __attribute__((ext_vector_type(2))) __bf16 bf16x2;
typedef __attribute__((ext_vector_type(4))) short s16x4;
typedef __attribute__((ext_vector_type(8))) short s16x8;
typedef __attribute__((ext_vector_type(16))) float f32x16;
typedef __attribute__((ext_vector_type(4))) float f32x4;
typedef __attribute__((ext_vector_type(2))) float f32x2;
typedef __attribute__((ext_vector_type(4))) unsigned u32x4;
typedef __attribute__((ext_vector_type(2))) unsigned u32x2;
typedef unsigned long long u64;
#define LAS __attribute__((address_space(3)))
#define GAS __attribute__((address_space(1)))
#define DI __device__ __forceinline__
#define MFMA(a, b, c) __builtin_amdgcn_mfma_f32_32x32x16_bf16((a), (b), (c), 0, 0, 0)
#define MFMA16(a, b, c) __builtin_amdgcn_mfma_f32_16x16x32_bf16((a), (b), (c), 0, 0, 0)
#define SB() __builtin_amdgcn_sched_barrier(0)
#define FENCE() do { asm volatile("" ::: "memory"); __builtin_amdgcn_sched_barrier(0); } while (0)
#define RLX_AGENT __ATOMIC_RELAXED, __HIP_MEMORY_SCOPE_AGENT

template <class F, int... I>
__device__ __forceinline__ void static_for_impl(F&& f, std::integer_sequence<int, I...>) { (f(std::integral_constant<int, I>{}), ...); }
template <int N, class F>
__device__ __forceinline__ void static_for(F&& f) { static_for_impl(f, std::make_integer_sequence<int, N>{}); }

constexpr int D_ = 448, F_ = 1792;
constexpr float LOG2E = 1.4426950408889634f;

constexpr unsigned WS_E = 0;
constexpr unsigned FSTRIDE = 384;
__host__ __device__ constexpr unsigned fl(unsigned line) { return (14u << 20) + line * FSTRIDE; }
constexpr int FL1 = 0, FL2 = 32, FL3 = 96, FL4 = 160;
constexpr unsigned WS_ST = 65536;
constexpr unsigned WS_KVP = WS_ST + (1u << 18);
constexpr unsigned WS_O = WS_KVP + (2u << 20);
constexpr unsigned WS_HB = WS_O + (2u << 20);
constexpr unsigned WS_HH = WS_HB + (2u << 20);
constexpr unsigned WS_BYTES = 16u << 20;

constexpr int SLOT = 28672;
constexpr int TAIL = 5 * SLOT;
constexpr int SM = 159744;
constexpr int L_CNT = SM;
constexpr int L_REL = SM + 128;
constexpr int L_SYNC = SM + 256;
constexpr int L_XRDY = SM + 272;
constexpr int L_BUSY = SM + 288;
constexpr int L_CS1 = SM + 512;
constexpr int L_TCH = SM + 304;
constexpr int L_CS4 = SM + 1024;
constexpr int L_WPH = SM + 2048;
constexpr int L_KVA = TAIL;
constexpr int L_RED = TAIL;
__host__ __device__ constexpr int xbuf(int c) { return (c % 5) == 0 ? 96 * 1024 : (c % 5) == 1 ? 112 * 1024 : ((c % 5) - 2) * 16384; }
__host__ __device__ constexpr int wbuf(int c) { return (c % 6) == 0 ? 88 * 1024 : 40 * 1024 + (c % 6) * 8192; }
constexpr int L_WQ = 128 * 1024;
constexpr int R_WQ = 27;
constexpr int LDS_BYTES = 163840;
constexpr int T_PITCH = 200;
constexpr int I_T = 7, I_WO = 8, I_A = 10, I_B = 14, I_D = 18, I_END = 26;
__host__ __device__ constexpr int slot_of(int idx) { return idx < 7 ? wbuf(idx) : ((idx - 7) % 5) * SLOT; }

DI void glds_s(unsigned voff, const void* sbase, unsigned lds_dst) {
  asm volatile("s_mov_b32 m0, %2\n\ts_nop 0\n\tglobal_load_lds_dwordx4 %0, %1" ::"v"(voff), "s"(sbase), "s"(lds_dst) : "memory");
}
DI void glds4_masked(unsigned voff, const void* sbase, unsigned lds_dst, u64 mask) {
  u64 keep;
  asm volatile("s_mov_b64 %0, exec\n\ts_mov_b64 exec, %4\n\ts_mov_b32 m0, %3\n\ts_nop 0\n\tglobal_load_lds_dword %1, %2\n\ts_mov_b64 exec, %0"
               : "=&s"(keep) : "v"(voff), "s"(sbase), "s"(lds_dst), "s"(mask) : "memory");
}
template <int N> DI void wait_v() { asm volatile("s_waitcnt vmcnt(%0)" ::"n"(N) : "memory"); }
DI bf16x8 tr_frag(LAS char* p0, LAS char* p1) {
  s16x4 a = __builtin_amdgcn_ds_read_tr16_b64_v4i16((LAS s16x4*)p0);
  s16x4 b = __builtin_amdgcn_ds_read_tr16_b64_v4i16((LAS s16x4*)p1);
  s16x8 r = __builtin_shufflevector(a, b, 0, 1, 2, 3, 4, 5, 6, 7);
  return __builtin_bit_cast(bf16x8, r);
}
DI unsigned pk2(float a, float b) {
  f32x2 v = {a, b};
  bf16x2 r = __builtin_convertvector(v, bf16x2);
  return __builtin_bit_cast(unsigned, r);
}
template <int S>
DI u32x4 pack8(const f32x16& x) {
  u32x4 p;
  p[0] = pk2(x[8 * S + 0], x[8 * S + 1]);
  p[1] = pk2(x[8 * S + 2], x[8 * S + 3]);
  p[2] = pk2(x[8 * S + 4], x[8 * S + 5]);
  p[3] = pk2(x[8 * S + 6], x[8 * S + 7]);
  return p;
}
DI bf16x8 asbf(const u32x4& u) { return __builtin_bit_cast(bf16x8, u); }
constexpr unsigned ONES2 = 0x3F803F80u;
DI void xhalf(float x, float& lo, float& hi) {
  unsigned u = __builtin_bit_cast(unsigned, x);
  auto r = __builtin_amdgcn_permlane32_swap(u, u, false, false);
  lo = __builtin_bit_cast(float, (unsigned)r[0]);
  hi = __builtin_bit_cast(float, (unsigned)r[1]);
}
DI float xsum(float x) { float a, b; xhalf(x, a, b); return a + b; }
DI float xmax(float x) { float a, b; xhalf(x, a, b); return fmaxf(a, b); }
DI float xmin(float x) { float a, b; xhalf(x, a, b); return fminf(a, b); }
DI float sel(bool c, float a, float b) { asm("" : "+v"(a), "+v"(b)); float r = c ? a : b; asm("" : "+v"(r)); return r; }
DI float bf_lo(unsigned u) { return __builtin_bit_cast(float, u << 16); }
DI float bf_hi(unsigned u) { return __builtin_bit_cast(float, u & 0xffff0000u); }

DI void dual_softmax(const f32x16& p, float sc, int hh, u32x4 (&o)[4]) {
  float mx = p[0], mn = p[0];
#pragma unroll
  for (int r = 1; r < 12; ++r) { mx = fmaxf(mx, p[r]); mn = fminf(mn, p[r]); }
  float mx2 = mx, mn2 = mn;
#pragma unroll
  for (int r = 12; r < 16; ++r) { mx2 = fmaxf(mx2, p[r]); mn2 = fminf(mn2, p[r]); }
  mx = hh ? mx : mx2; mn = hh ? mn : mn2;
  mx = xmax(mx); mn = xmin(mn);
  const float a = -mx * sc, b = mn * sc;
  float sp0 = 0.f, sn0 = 0.f, sp1 = 0.f, sn1 = 0.f;
  f32x16 ep, en;
#pragma unroll
  for (int r = 0; r < 16; ++r) {
    float e1 = __builtin_amdgcn_exp2f(fmaf(p[r], sc, a));
    float e2 = __builtin_amdgcn_exp2f(fmaf(p[r], -sc, b));
    if (r >= 12) { e1 = hh ? 0.f : e1; e2 = hh ? 0.f : e2; }
    ep[r] = e1; en[r] = e2;
    if (r & 1) { sp1 += e1; sn1 += e2; } else { sp0 += e1; sn0 += e2; }
  }
  const float sp = xsum(sp0 + sp1), sn = xsum(sn0 + sn1);
  const float rp = __builtin_amdgcn_rcpf(sp), rn = __builtin_amdgcn_rcpf(sn);
#pragma unroll
  for (int r = 0; r < 16; ++r) { ep[r] *= rp; en[r] *= rn; }
  o[0] = pack8<0>(ep); o[1] = pack8<1>(ep); o[2] = pack8<0>(en); o[3] = pack8<1>(en);
}

DI u32x4 ld_sc1(__amdgpu_buffer_rsrc_t rs, unsigned voff, unsigned soff) { return __builtin_bit_cast(u32x4, __builtin_amdgcn_raw_buffer_load_b128(rs, voff, soff, 16)); }
DI f32x2 ld8_sc1(__amdgpu_buffer_rsrc_t rs, unsigned voff, unsigned soff) { return __builtin_bit_cast(f32x2, __builtin_amdgcn_raw_buffer_load_b64(rs, voff, soff, 16)); }
DI void st_sc1(__amdgpu_buffer_rsrc_t rs, const u32x4& v, unsigned voff, unsigned soff) { __builtin_amdgcn_raw_buffer_store_b128(v, rs, voff + soff, 0, 16); }
DI void st8_sc1(__amdgpu_buffer_rsrc_t rs, const u32x2& v, unsigned voff, unsigned soff) { __builtin_amdgcn_raw_buffer_store_b64(v, rs, voff, soff, 16); }
DI void wait_flags(char* ws, unsigned off, bool active, u64 tag) {
  GAS u64* p = (GAS u64*)(ws + off);
  GAS u64* ab = (GAS u64*)(ws + WS_E + 128);
  for (unsigned spins = 0;; ++spins) {
    u64 v = tag;
    if (active) v = __hip_atomic_load(p, RLX_AGENT);
    if (__all(v == tag)) break;
    __builtin_amdgcn_s_sleep(1);
    if ((spins & 63u) == 63u) {
      if (spins > 20000u) { __hip_atomic_store(ab, tag, RLX_AGENT); break; }
      if (__hip_atomic_load(ab, RLX_AGENT) == tag) break;
    }
  }
  FENCE();
}
DI void set_flag(char* ws, unsigned off, u64 tag, int lane) {
  asm volatile("s_waitcnt vmcnt(0)" ::: "memory");
  if (lane == 0) __hip_atomic_store((GAS u64*)(ws + off), tag, RLX_AGENT);
}

template <int NP, class Rdy, class Load>
DI void gather(Rdy rdy, Load load) {
  constexpr unsigned ALL = (1u << NP) - 1u;
  unsigned done = 0;
  for (unsigned spins = 0; spins < 20000u; ++spins) {
    const unsigned nw = (unsigned)__ballot(rdy()) & ALL & ~done;
    static_for<NP>([&](auto pc) __attribute__((always_inline)) { if ((nw >> decltype(pc)::value) & 1u) load(pc); });
    done |= nw;
    if (done == ALL) break;
    if (nw == 0) __builtin_amdgcn_s_sleep(1);
  }
  FENCE();
}

constexpr unsigned LSPIN = 1u << 18;
constexpr int LA3 = 8, LA4 = 10, LA5 = 14;
constexpr int EARLY5 = 12;
constexpr int WB5 = 24;
DI unsigned rfl(unsigned v) { return __builtin_amdgcn_readfirstlane(v); }
DI unsigned min4(const u32x4& v) { unsigned a = v[0] < v[1] ? v[0] : v[1], b = v[2] < v[3] ? v[2] : v[3]; return rfl(a < b ? a : b); }
DI u32x4 rd4(LAS char* p) { return *(volatile LAS u32x4*)p; }
DI unsigned rd1(LAS char* p) { return *(volatile LAS unsigned*)p; }
DI void wr1(LAS char* p, unsigned v) { *(volatile LAS unsigned*)p = v; }
DI void spin4(LAS char* p, unsigned need) {
  for (unsigned s = 0; s < LSPIN; ++s) { asm volatile("" : "+s"(s)); if (min4(rd4(p)) >= need) break; }
  FENCE();
}
DI void spin1(LAS char* p, unsigned need) {
  for (unsigned s = 0; s < LSPIN; ++s) { asm volatile("" : "+s"(s)); if (rfl(rd1(p)) >= need) break; }
  FENCE();
}
#define LDS_DONE() asm volatile("s_waitcnt lgkmcnt(0)" ::: "memory")
DI void post(LAS char* p, unsigned v) { FENCE(); LDS_DONE(); wr1(p, v); FENCE(); }
DI void add1(unsigned lds_addr, int v) {
  u64 keep;
  FENCE();
  asm volatile("s_mov_b64 %0, exec\n\ts_mov_b64 exec, 1\n\tds_add_u32 %1, %2\n\ts_mov_b64 exec, %0" : "=&s"(keep) : "v"(lds_addr), "v"(v) : "memory");
  FENCE();
}

#define LANE_VARS() int lane = __builtin_amdgcn_mbcnt_hi(~0u, __builtin_amdgcn_mbcnt_lo(~0u, 0u)); asm volatile("" : "+v"(lane)); \
  const int t32 = lane & 31, hh = lane >> 5, blk = (lane >> 4) & 1, q4 = (lane & 15) >> 2, p4 = lane & 3; \
  (void)t32; (void)hh; (void)blk; (void)q4; (void)p4
#define O128_VARS() \
  const int o128a = (4 * hh + q4) * 128 + 8 * (p4 & 1) + (((2 * blk + (p4 >> 1)) ^ wrow_swz(q4)) * 16); \
  const int o128b = (4 * hh + q4) * 128 + 8 * (p4 & 1) + (((4 + 2 * blk + (p4 >> 1)) ^ wrow_swz(q4)) * 16)
DI int wrow_swz(int row) { return ((row >> 1) & 1) << 2; }
__host__ __device__ constexpr int p5_ftile(int i) { return i < 28 ? 8 * (i >> 2) + (i & 3) : 8 * ((i - 28) >> 2) + 4 + (i & 3); }

DI void center(f32x16& a, const f32x4 (&c)[4], float nmu) {
#pragma unroll
  for (int g = 0; g < 4; ++g)
#pragma unroll
    for (int r = 0; r < 4; ++r) a[4 * g + r] = fmaf(c[g][r], nmu, a[4 * g + r]);
}
DI u32x4 glu_tile(const f32x16& v, LAS char* cs, float nmu, float k1, float sg) {
  f32x4 c[4];
#pragma unroll
  for (int g = 0; g < 4; ++g) c[g] = *(LAS f32x4*)(cs + 32 * g);
  const f32x2 nmu2 = {nmu, nmu}, k12 = {k1, k1}, sg2 = {sg, sg};
  u32x4 o;
#pragma unroll
  for (int r = 0; r < 4; ++r) {
    const f32x2 cg = {c[r >> 1][2 * (r & 1)], c[r >> 1][2 * (r & 1) + 1]}, cu = {c[2 + (r >> 1)][2 * (r & 1)], c[2 + (r >> 1)][2 * (r & 1) + 1]};
    const f32x2 g = __builtin_elementwise_fma(cg, nmu2, (f32x2){v[2 * r], v[2 * r + 1]}) * k12;
    const f32x2 u = __builtin_elementwise_fma(cu, nmu2, (f32x2){v[8 + 2 * r], v[9 + 2 * r]});
    const f32x2 e = {__builtin_amdgcn_exp2f(g[0]), __builtin_amdgcn_exp2f(g[1])};
    const f32x2 d = __builtin_elementwise_fma(e, sg2, sg2);
    const f32x2 y = u * (f32x2){__builtin_amdgcn_rcpf(d[0]), __builtin_amdgcn_rcpf(d[1])};
    o[r] = pk2(y[0], y[1]);
  }
  return o;
}
__global__ __launch_bounds__(512, 2) void fused_block(const __bf16* __restrict__ x, const __bf16* __restrict__ wqkv,
                                                      const __bf16* __restrict__ wphi, const __bf16* __restrict__ wo,
                                                      const __bf16* __restrict__ wg, const __bf16* __restrict__ wu,
                                                      const __bf16* __restrict__ wd, char* ws, __bf16* __restrict__ out) {
  extern __shared__ __attribute__((aligned(16))) char smem_[];
  asm volatile("" ::"s"(x), "s"(wqkv), "s"(wphi), "s"(wo), "s"(wg), "s"(wu), "s"(wd), "s"(ws), "s"(out));
  LAS char* lds = (LAS char*)smem_;
  const unsigned lbase = (unsigned)(size_t)lds;
  const int tid = threadIdx.x, lane = tid & 63;
  const int w = __builtin_amdgcn_readfirstlane(tid >> 6);
  const int bid = blockIdx.x, g8 = bid & 7, r32 = bid >> 3;
  const int t32 = lane & 31, hh = lane >> 5, blk = (lane >> 4) & 1, q4 = (lane & 15) >> 2, p4 = lane & 3;
  const int m1 = (g8 & 3) * 4 + (r32 >> 3), h = (g8 >> 2) * 8 + (r32 & 7), hodd = h & 1;
  const int mp = g8 * 4 + (r32 & 3), jn = r32 >> 2;
  const bool act35 = jn < 7;
  const int m4 = g8 * 2 + (r32 >= 14), j4 = r32 >= 14 ? r32 - 14 : r32;
  const int rot = (h + 3 * m1) % 7;
  unsigned e0, e1;
  const char* eb;
  if (w < 4) {
    e0 = (32 * w + (lane >> 3)) * (D_ * 2) + (((lane & 7) ^ (lane >> 4)) * 16);
    e1 = (32 * w + 8 + (lane >> 3)) * (D_ * 2) + (((lane & 7) ^ (4 + (lane >> 4))) * 16);
    eb = (const char*)x + (size_t)m1 * 128 * (D_ * 2);
  } else {
    {
      const int c = (lane & 7) ^ wrow_swz(lane >> 3);
      e0 = (16 * (w - 4) + (lane >> 3)) * (3 * D_ * 2) + (1 + (c >> 2)) * (D_ * 2) + (c & 3) * 16;
      e1 = e0 + 8 * (3 * D_ * 2);
    }
    eb = (const char*)wqkv + (56 * h - 8 * hodd);
  }
  auto x_issue = [&](int cs_) {
    const int c = cs_ + rot >= 7 ? cs_ + rot - 7 : cs_ + rot;
#pragma unroll
    for (int i = 0; i < 2; ++i) {
      glds_s(e0, eb + c * 128 + i * 16 * (D_ * 2), lbase + xbuf(cs_) + (4 * w + 2 * i) * 1024);
      glds_s(e1, eb + c * 128 + i * 16 * (D_ * 2), lbase + xbuf(cs_) + (4 * w + 2 * i + 1) * 1024);
    }
  };
  auto p1_issue = [&](int cs_) {
    const int c = cs_ + rot >= 7 ? cs_ + rot - 7 : cs_ + rot;
    const char* b = eb + (size_t)(c * 64) * (3 * D_ * 2);
    const unsigned d = lbase + slot_of(cs_) + 2 * (w - 4) * 1024;
    glds_s(e0, b, d); glds_s(e1, b, d + 1024);
  };
  if (w < 4) { x_issue(0); x_issue(1); } else { p1_issue(0); p1_issue(1); }
  if (w >= 4) {
    if (w == 7) *(LAS u32x2*)(lds + SM + lane * 8) = (u32x2){0u, 0u};
    *(LAS u32x2*)(lds + L_WPH + ((w - 4) * 64 + lane) * 8) = (u32x2){0u, 0u};
    LDS_DONE();
  }
  __builtin_amdgcn_s_barrier();
  FENCE();
  const int onesoff = (16 * (w - 4) + (lane & 15)) * 128 + (((hodd ? 4 : 7) ^ wrow_swz(lane & 15)) * 16) + (hodd ? 0 : 8);
  if (w >= 4) {
    wait_v<2>();
    *(volatile LAS unsigned short*)(lds + wbuf(0) + onesoff) = 0x3F80;
    LDS_DONE();
    add1(lbase + L_CNT, 1);
    p1_issue(2);
  }
  const __amdgpu_buffer_rsrc_t rs = __builtin_amdgcn_make_buffer_rsrc((void*)ws, 0, WS_BYTES, 0x00020000);
  auto landed = [&](int e) { spin1(lds + L_CNT + 4 * e, 4); };
  auto release = [&](int e) { add1(lbase + L_REL + 4 * e, 1); };

  if (w >= 4) {
    const int l = w - 4;
    LANE_VARS();
    const unsigned e0 = (16 * l + (lane >> 3)) * (3 * D_ * 2) + (1 + (((lane & 7) ^ wrow_swz(lane >> 3)) >> 2)) * (D_ * 2) + (lane & 3) * 16;
    const unsigned e1 = e0 + 8 * (3 * D_ * 2), e2 = (lane >> 2) * (3 * D_ * 2) + (lane & 3) * 16;
    const int onesoff = (16 * l + (lane & 15)) * 128 + (((hodd ? 4 : 7) ^ wrow_swz(lane & 15)) * 16) + (hodd ? 0 : 8);
    auto p1_issue = [&](int cs_) {
      const int c = cs_ + rot >= 7 ? cs_ + rot - 7 : cs_ + rot;
      const char* b = eb + (size_t)(c * 64) * (3 * D_ * 2);
      const unsigned d = lbase + slot_of(cs_) + 2 * l * 1024;
      glds_s(e0, b, d); glds_s(e1, b, d + 1024);
    };
#pragma unroll
    for (int i = 0; i < 2; ++i) {
      const int g = i == 0 ? l : (l + 4 < 7 ? l + 4 : 6);
      glds4_masked((h * 28 + 4 * g + (lane >> 4)) * 56 + ((lane & 15) < 14 ? (lane & 15) : 0) * 4, wphi, lbase + L_WPH + (hodd + g) * 256, 0x3FFF3FFF3FFF3FFFull);
    }
    const unsigned v128 = (lane >> 3) * (D_ * 2) + (((lane & 7) ^ wrow_swz(lane >> 3)) * 16) + (act35 ? jn : 0) * 128;
    const unsigned vgu = ((l & 1) * 56 + (lane >> 3)) * (F_ * 2) + (((lane & 7) ^ wrow_swz(lane >> 3) ^ (l & 2)) * 16) + j4 * 256;
    const char* gub = (const char*)(l < 2 ? wg : wu);
    unsigned pub = 1;
    auto publish = [&](unsigned v) {
      while (pub < v) {
        if (pub < 7) { *(volatile LAS unsigned short*)(lds + wbuf(pub) + onesoff) = 0x3F80; LDS_DONE(); }
        add1(lbase + L_CNT + 4 * pub, 1); ++pub;
      }
    };
    auto prev_of = [&](int idx) {
      if (idx < 7) return idx - 6;
      if (idx < 12) return idx == I_WO ? 4 : idx == I_WO + 1 ? 5 : idx == I_A ? 6 : R_WQ;
      const int p = idx - 5;
      return p;
    };
    auto nrel = [&](int p) { return (p < I_T || (p >= I_A && p < I_D) || p == R_WQ) ? 8u : 4u; };
    auto chk_free = [&](int idx, unsigned seen) {
      const int p = prev_of(idx);
      if (rfl(seen) < nrel(p)) { wait_v<0>(); publish(idx); spin1(lds + L_REL + 4 * p, nrel(p)); }
      FENCE();
    };
    auto hold = [&](unsigned seen, int idx) {
      if (rfl(seen) != 0) { wait_v<0>(); publish(idx); for (unsigned sp = 0; sp < LSPIN; ++sp) { if (rfl(rd1(lds + L_BUSY)) == 0) break; } }
      FENCE();
    };
    auto wo_g = [&](int n, int i) {
      glds_s(v128, (const char*)wo + (size_t)(n * 28 + l * 7 + i) * 8 * (D_ * 2), lbase + slot_of(I_WO + n) + (l * 7 + i) * 1024);
    };
    auto g_g = [&](int n, int i) {
      glds_s(vgu, gub + (size_t)((n & 3) * 112 + 8 * i) * (F_ * 2) + (n >> 2) * 128, lbase + slot_of(I_A + n) + (l * 7 + i) * 1024);
    };
    auto d_g = [&](int n, int i) {
      const int r0 = 16 * p5_ftile(n * 7 + (i >> 1)) + 8 * (i & 1), r1 = 16 * p5_ftile(n * 7 + ((7 + i) >> 1)) + 8 * ((7 + i) & 1);
      glds_s(v128, (const char*)wd + (size_t)((l >> 1) * 896 + ((l & 1) ? r1 : r0)) * (D_ * 2), lbase + slot_of(I_D + n) + (l * 7 + i) * 1024);
    };
    const bf16x8 ones = asbf((u32x4){ONES2, ONES2, ONES2, ONES2}), half1 = asbf((u32x4){ONES2, ONES2, 0u, 0u});
    const int g4 = lane >> 4;
    f32x4 c0 = {}, c1 = {};
    bf16x8 ca[4], cb[4];
    const int ogu = (4 * g4 + q4) * 128 + 8 * (p4 & 1);
    const int oga = ogu + (((2 * l + (p4 >> 1)) ^ wrow_swz(q4)) * 16), oua = ogu + 14336 + (((2 * l + (p4 >> 1)) ^ wrow_swz(q4) ^ 2) * 16);
    auto nkb_of = [&](int e) { return e < I_T ? 2 : 4; };
    auto cs_rd = [&](int e, unsigned seen) {
      if (rfl(seen) < 4) spin1(lds + L_CNT + 4 * e, 4);
      FENCE();
      LAS char* p = lds + slot_of(e);
      if (e >= I_A) {
#pragma unroll
        for (int kb = 0; kb < 3; ++kb) {
          ca[kb] = tr_frag(p + oga + kb * 4096, p + oga + kb * 4096 + 2048);
          cb[kb] = tr_frag(p + oua + kb * 4096, p + oua + kb * 4096 + 2048);
        }
        ca[3] = tr_frag(p + oga + 12288, p + oga + 12288);
        cb[3] = tr_frag(p + oua + 12288, p + oua + 12288);
      } else {
#pragma unroll
        for (int kb = 0; kb < 2; ++kb) ca[kb] = tr_frag(p + oga + kb * 4096, p + oga + kb * 4096 + 2048);
      }
    };
    auto cs_mm = [&](int e) {
#pragma unroll
      for (int kb = 0; kb < nkb_of(e); ++kb) {
        const bf16x8 o = (e >= I_A && kb == 3) ? half1 : ones;
        c0 = MFMA16(ca[kb], o, c0);
        if (e >= I_A) c1 = MFMA16(cb[kb], o, c1);
      }
      LDS_DONE();
      add1(lbase + L_REL + 4 * e, 1);
    };
#define STEP(idx, N, G, e, HOLD, WV, pubto) do {                                              \
      const unsigned rf_ = (idx) >= 6 ? rd1(lds + L_REL + 4 * prev_of(idx)) : 0u;             \
      const unsigned rc_ = (e) >= 0 ? rd1(lds + L_CNT + 4 * ((e) >= 0 ? (e) : 0)) : 0u;       \
      unsigned rb_ = HOLD ? rd1(lds + L_BUSY) : 0u;                                           \
      if ((e) >= 0) cs_rd((e), rc_);                                                          \
      if ((idx) >= 6) chk_free((idx), rf_);                                                   \
      if (HOLD) { hold(rb_, (idx)); rb_ = rd1(lds + L_BUSY); }                                       \
      _Pragma("unroll") for (int i_ = 0; i_ < (N); ++i_) { if (HOLD && i_ == 4) hold(rb_, (idx)); G; } \
      if ((e) >= 0) cs_mm(e);                                                                 \
      wait_v<WV>(); publish(pubto);                                                           \
    } while (0)
#define TAILCS(e) do { cs_rd((e), rd1(lds + L_CNT + 4 * (e))); cs_mm(e); } while (0)
    STEP(3, 1, p1_issue(3), -1, false, 6, 2);
    STEP(4, 1, p1_issue(4), -1, false, 6, 3);
    STEP(5, 1, p1_issue(5), 0, false, 4, 4);
    STEP(6, 1, p1_issue(6), 1, false, 4, 5);
    {
      auto cs2 = [&](int e, int o) {
        ca[o] = tr_frag(lds + slot_of(e) + oga, lds + slot_of(e) + oga + 2048);
        ca[o + 1] = tr_frag(lds + slot_of(e) + oga + 4096, lds + slot_of(e) + oga + 6144);
      };
      landed(4);
      cs2(2, 0); cs2(3, 2);
      cb[0] = tr_frag(lds + slot_of(4) + oga, lds + slot_of(4) + oga + 2048);
      cb[1] = tr_frag(lds + slot_of(4) + oga + 4096, lds + slot_of(4) + oga + 6144);
      FENCE();
#pragma unroll
      for (int i = 0; i < 7; ++i) {
        const int c = i + rot >= 7 ? i + rot - 7 : i + rot;
        glds_s(e2, eb + (size_t)(c * 64 + 16 * l) * (3 * D_ * 2), lbase + L_WQ + (i * 4 + l) * 1024);
        if (i == 0) { LDS_DONE(); add1(lbase + L_REL + 8, 1); add1(lbase + L_REL + 12, 1); add1(lbase + L_REL + 16, 1); }
        if (i == 1) { wait_v<4>(); publish(6); }
        if (i == 3) { wait_v<4>(); publish(7); }
      }
#pragma unroll
      for (int k = 0; k < 4; ++k) c0 = MFMA16(ca[k], ones, c0);
      c1 = MFMA16(cb[0], ones, c1); c1 = MFMA16(cb[1], ones, c1);
      FENCE();
      landed(6);
      cs2(5, 0); cs2(6, 2);
      c0 = MFMA16(ca[0], ones, c0); c1 = MFMA16(ca[1], ones, c1); c0 = MFMA16(ca[2], ones, c0); c1 = MFMA16(ca[3], ones, c1);
      c0 += c1;
    }
    if ((lane & 15) == 0) *(LAS f32x4*)(lds + L_CS1 + (32 + l * 16 + 4 * g4) * 4) = c0;
    LDS_DONE();
    add1(lbase + L_XRDY, 1);
    add1(lbase + L_REL + 20, 1); add1(lbase + L_REL + 24, 1);
    wait_v<0>(); publish(I_WO);
    if (l < 2) {
      spin1(lds + L_CNT + 4 * 7, 4);
      LAS char* p = lds + L_WQ + (4 * g4 + q4) * 64 + l * 32 + p4 * 8;
      c0 = (f32x4){}; c1 = (f32x4){};
#pragma unroll
      for (int kb = 0; kb < 14; kb += 2) {
        c0 = MFMA16(tr_frag(p + kb * 2048, p + kb * 2048 + 1024), ones, c0);
        c1 = MFMA16(tr_frag(p + kb * 2048 + 2048, p + kb * 2048 + 3072), ones, c1);
      }
      if ((lane & 15) == 0) *(LAS f32x4*)(lds + L_CS1 + (l * 16 + 4 * g4) * 4) = c0 + c1;
    }
    LDS_DONE();
    add1(lbase + L_XRDY + 12, 1);
    add1(lbase + L_REL + 4 * R_WQ, 1);
    c0 = (f32x4){}; c1 = (f32x4){};
    if (act35) {
      STEP(I_WO, 7, wo_g(0, i_), -1, true, 63, 0);
      STEP(I_WO + 1, 7, wo_g(1, i_), -1, true, 63, 0);
    }
    {
      const int nl_ = l < 3 ? 49 : 13;
      const unsigned tv = l < 3 ? bid * 6272 + (lane < nl_ ? lane : 0) * 128 : (bid * 1568 + (lane < nl_ ? lane : 0) * 128) & ~15u;
      glds4_masked(tv, l == 0 ? wd : l == 1 ? wu : l == 2 ? wg : wo, lbase + L_TCH, (1ull << 49) - 1);
    }
    if (act35) {
      auto cs_out = [&](int part) {
        if ((lane & 15) == 0) {
          *(LAS f32x4*)(lds + L_CS4 + ((part * 4 + l) * 32 + 4 * g4) * 4) = c0;
          *(LAS f32x4*)(lds + L_CS4 + ((part * 4 + l) * 32 + 16 + 4 * g4) * 4) = c1;
        }
        LDS_DONE();
        add1(lbase + L_XRDY + 4 + 4 * part, 1);
        c0 = (f32x4){}; c1 = (f32x4){};
      };
      STEP(I_A, 7, g_g(0, i_), -1, true, 7, I_A);
      STEP(I_A + 1, 7, g_g(1, i_), -1, true, 63, 0);
      STEP(I_A + 2, 7, g_g(2, i_), -1, true, 14, I_A + 1);
      STEP(I_A + 3, 7, g_g(3, i_), I_A, true, 14, I_A + 2);
      STEP(I_B, 7, g_g(4, i_), I_A + 1, true, 14, I_A + 3);
      TAILCS(I_A + 2);
      wait_v<7>(); publish(I_B); TAILCS(I_A + 3);
      cs_out(0);
      wait_v<0>(); publish(I_B + 1); TAILCS(I_B);
      STEP(I_B + 1, 7, g_g(5, i_), -1, false, 63, 0);
      STEP(I_B + 2, 7, g_g(6, i_), -1, false, 7, I_B + 2);
      STEP(I_B + 3, 7, g_g(7, i_), I_B + 1, false, 7, I_B + 3);
      STEP(I_D, 7, d_g(0, i_), I_B + 2, false, 7, I_D);
      STEP(I_D + 1, 7, d_g(1, i_), I_B + 3, false, 63, 0);
      cs_out(1);
#pragma unroll
      for (int n = 2; n < 8; ++n) STEP(I_D + n, 7, d_g(n, i_), -1, false, 14, I_D + n - 1);
      wait_v<7>(); publish(I_END - 1);
      wait_v<0>(); publish(I_END);
    } else {
      wait_v<0>();
    }
    return;
  }

  const int kh = w & 1, tt = w >> 1;
  const int ttp = mp * 2 + tt, ttg = m4 * 4 + w, nt3 = jn * 2 + kh;
  const unsigned so_o = WS_O + (((m1 * 4 + w) * 16 + h) * 2) * 1024;
  const unsigned so_h = WS_HB + (ttp * 28 + nt3 * 2) * 1024, so_s = WS_ST + (ttp * 14 + nt3) * 256;
  const unsigned so_g = WS_HH + (ttg * 112 + j4 * 8) * 1024;
  const u64 epoch_v = __hip_atomic_load((GAS u64*)(ws + WS_E), RLX_AGENT);
  LAS char* mysync = lds + L_SYNC + 4 * w;

  f32x16 ak = {}, av = {}, axx = {};
  f32x16 pk = {}, kv = {}, aq = {};
  const int xrow = w * 32 + t32;
  f32x4 ck[4], cv[4];
  bf16x8 wph0, wph1;
  bf16x8 fx[28];
  {
    int xo[4];
#pragma unroll
    for (int s = 0; s < 4; ++s) xo[s] = xrow * 128 + (((2 * s + hh) ^ ((xrow >> 1) & 7)) * 16);
    const int wok_ = (8 * hh + q4) * 128 + 8 * (p4 & 1) + (((2 * blk + (p4 >> 1)) ^ wrow_swz(q4)) * 16);
    const int wov_ = (8 * hh + q4) * 128 + 8 * (p4 & 1) + (((4 + 2 * blk + (p4 >> 1)) ^ wrow_swz(q4)) * 16);
    bf16x8 fk[2], fv[2];
    auto px = [&](int i) { return lds + xbuf(i >> 2) + xo[i & 3]; };
    auto pw = [&](int i) { return lds + slot_of(i >> 2) + (i & 3) * 2048; };
    wait_v<4>();
    landed(0);
#pragma unroll
    for (int i = 0; i < 2; ++i) {
      fx[i] = *(LAS bf16x8*)px(i);
      fk[i] = tr_frag(pw(i) + wok_, pw(i) + wok_ + 512); fv[i] = tr_frag(pw(i) + wov_, pw(i) + wov_ + 512);
    }
    unsigned nl = rd1(lds + L_CNT + 4);
    FENCE();
    x_issue(2);
#pragma unroll
    for (int i = 0; i < 28; ++i) {
      const int j = i + 2, b = i & 1;
      if (j < 28 && (j & 3) == 0) {
        if (j <= 16) wait_v<8>(); else if (j == 20) wait_v<4>(); else wait_v<0>();
        if (rfl(nl) < 4) landed(j >> 2);
        FENCE();
      }
      if (i == 27) {
        if (rfl(nl) < 4) spin1(lds + L_XRDY, 4);
        FENCE();
#pragma unroll
        for (int g = 0; g < 4; ++g) {
          ck[g] = *(LAS f32x4*)(lds + L_CS1 + (32 + 4 * hh + 8 * g) * 4);
          cv[g] = *(LAS f32x4*)(lds + L_CS1 + (64 + 4 * hh + 8 * g) * 4);
        }
        LAS char* wpp = lds + L_WPH + (4 * hh + q4) * 64 + blk * 32 + p4 * 8;
        wph0 = tr_frag(wpp, wpp + 512); wph1 = tr_frag(wpp + 1024, wpp + 1536);
        FENCE();
      }
      axx = MFMA(fx[i], fx[i], axx);
      if (j < 28) fx[j] = *(LAS bf16x8*)px(j);
      if (j < 24 && (j & 3) == 2 && j > 2) nl = rd1(lds + L_CNT + 4 * ((j >> 2) + 1));
      if (i == 26) nl = rd1(lds + L_XRDY);
      SB();
      av = MFMA(fv[b], fx[i], av);
      if (j < 28) fv[b] = tr_frag(pw(j) + wov_, pw(j) + wov_ + 512);
      SB();
      if ((i & 3) == 3) { asm volatile("" ::"v"(fk[b]), "v"(fx[i])); release(i >> 2); }
      ak = MFMA(fk[b], fx[i], ak);
      if (j < 28) fk[b] = tr_frag(pw(j) + wok_, pw(j) + wok_ + 512);
      FENCE();
      if (i == 0) x_issue(3);
      if (i == 3 || i == 7 || i == 11) x_issue(4 + (i >> 2));
      if (i == 24) spin1(lds + L_REL + 12, 8);
    }
  }
  const u64 tag = (((u64)rfl((unsigned)(epoch_v >> 32)) << 32) | (u64)rfl((unsigned)epoch_v)) + 1;
  float nmu, rstd;
  {
    const bool b0 = t32 & 1, b1 = t32 & 2, b3 = t32 & 8, b4 = t32 & 16;
    const float d0 = sel(b1, sel(b0, axx[3], axx[2]), sel(b0, axx[1], axx[0])), d1 = sel(b1, sel(b0, axx[7], axx[6]), sel(b0, axx[5], axx[4]));
    const float d2 = sel(b1, sel(b0, axx[11], axx[10]), sel(b0, axx[9], axx[8])), d3 = sel(b1, sel(b0, axx[15], axx[14]), sel(b0, axx[13], axx[12]));
    float dg = sel(b4, sel(b3, d3, d2), sel(b3, d1, d0));
    dg = (((t32 >> 2) & 1) == hh) ? dg : 0.f;
    float slo, shi;
    xhalf(hodd ? av[0] : av[12], slo, shi);
    const float sq = xsum(dg), mu = (hodd ? slo : shi) * (1.0f / D_);
    nmu = -mu;
    rstd = __builtin_amdgcn_rsqf(fmaxf(sq * (1.0f / D_) - mu * mu, 0.f) + 1e-5f);
  }
  LAS char* T = lds + slot_of(I_T);
  {
    center(ak, ck, nmu);
    pk = MFMA(wph0, asbf(pack8<0>(ak)), pk);
    pk = MFMA(wph1, asbf(pack8<1>(ak)), pk);
    center(av, cv, nmu);
#pragma unroll
    for (int r = 0; r < 16; ++r) av[r] *= rstd;
    av[0] = (hodd && hh == 0) ? 1.0f : av[0];
    av[12] = (!hodd && hh == 1) ? 1.0f : av[12];
    const u32x4 v0 = pack8<0>(av), v1 = pack8<1>(av);
    LAS char* tp = T + xrow * T_PITCH + 8 * hh;
#pragma unroll
    for (int g = 0; g < 4; ++g) {
      const u32x4& c = (g >> 1) ? v1 : v0;
      *(LAS u32x2*)(tp + 128 + 16 * g) = (u32x2){c[(g & 1) * 2], c[(g & 1) * 2 + 1]};
    }
    u32x4 ph[4];
    dual_softmax(pk, rstd * LOG2E, hh, ph);
#pragma unroll
    for (int g = 0; g < 4; ++g) {
      const u32x4& a = ph[g >> 1]; const u32x4& b = ph[2 + (g >> 1)];
      const int e = (g & 1) * 2;
      *(LAS u32x2*)(tp + 16 * g) = (u32x2){a[e], a[e + 1]};
      *(LAS u32x2*)(tp + 64 + 16 * g) = (u32x2){b[e], b[e + 1]};
    }
    post(mysync, 1);
  }
  if (w < 2) {
    add1(lbase + L_BUSY, 1);
    spin4(lds + L_SYNC, 1);
    LAS char* ap = T + (8 * hh + q4) * T_PITCH + blk * 32 + p4 * 8;
    bf16x8 fa[8], fb[8];
#pragma unroll
    for (int s = 0; s < 8; ++s) {
      LAS char* p = ap + s * 16 * T_PITCH;
      fa[s] = tr_frag(p + w * 64, p + w * 64 + 4 * T_PITCH);
      fb[s] = tr_frag(p + 128, p + 128 + 4 * T_PITCH);
    }
#pragma unroll
    for (int s = 0; s < 8; ++s) kv = MFMA(fa[s], fb[s], kv);
    const unsigned so = WS_KVP + ((h * 16 + m1) * 4 + w * 2) * 1024;
    st_sc1(rs, pack8<0>(kv), lane * 16, so);
    st_sc1(rs, pack8<1>(kv), lane * 16 + 1024, so);
  }
  LDS_DONE();
  release(I_T);
  u32x4 phq[4];
  {
    landed(7);
    LAS char* pq_ = lds + L_WQ + (8 * hh + q4) * 64 + blk * 32 + p4 * 8;
    bf16x8 fq[4];
#pragma unroll
    for (int i = 0; i < 4; ++i) fq[i] = tr_frag(pq_ + i * 1024, pq_ + i * 1024 + 256);
    FENCE();
#pragma unroll
    for (int i = 0; i < 28; ++i) {
      if (i == 27) { asm volatile("" ::"v"(fq[3])); release(R_WQ); }
      aq = MFMA(fq[i & 3], fx[i], aq);
      if (i + 4 < 28) fq[i & 3] = tr_frag(pq_ + (i + 4) * 1024, pq_ + (i + 4) * 1024 + 256);
      FENCE();
      if (i == 10 && w < 2) { set_flag(ws, fl(FL1 + h * 2 + w) + m1 * 8, tag, lane); add1(lbase + L_BUSY, -1); }
    }
    spin1(lds + L_XRDY + 12, 4);
    f32x4 cq[4];
#pragma unroll
    for (int g = 0; g < 4; ++g) cq[g] = *(LAS f32x4*)(lds + L_CS1 + (4 * hh + 8 * g) * 4);
    center(aq, cq, nmu);
    f32x16 pq = {};
    pq = MFMA(wph0, asbf(pack8<0>(aq)), pq);
    pq = MFMA(wph1, asbf(pack8<1>(aq)), pq);
    dual_softmax(pq, rstd * LOG2E, hh, phq);
  }
  {
    u32x4 v[16];
    f32x16 num = {};
    GAS u64* f1 = (GAS u64*)(ws + fl(FL1 + h * 2 + (w >> 1)) + (lane & 15) * 8);
    gather<16>([&]() { return __hip_atomic_load(f1, RLX_AGENT) == tag; },
               [&](auto pc) { v[decltype(pc)::value] = ld_sc1(rs, lane * 16, WS_KVP + ((h * 16 + decltype(pc)::value) * 4 + w) * 1024); });
    float sm[8] = {};
#pragma unroll
    for (int i = 0; i < 16; ++i)
#pragma unroll
      for (int k = 0; k < 4; ++k) { const unsigned u = v[i][k]; sm[2 * k] += bf_lo(u); sm[2 * k + 1] += bf_hi(u); }
    spin1(lds + L_REL + 4 * R_WQ, 8);
    *(LAS u32x4*)(lds + L_KVA + w * 1024 + lane * 16) = (u32x4){pk2(sm[0], sm[1]), pk2(sm[2], sm[3]), pk2(sm[4], sm[5]), pk2(sm[6], sm[7])};
    post(mysync, 2);
    spin4(lds + L_SYNC, 2);
    bf16x8 ka[4];
#pragma unroll
    for (int i = 0; i < 4; ++i) ka[i] = *(LAS bf16x8*)(lds + L_KVA + i * 1024 + lane * 16);
#pragma unroll
    for (int i = 0; i < 4; ++i) num = MFMA(ka[i], asbf(phq[i]), num);
    float lo, hi;
    xhalf(hodd ? num[0] : num[12], lo, hi);
    const float rd = __builtin_amdgcn_rcpf((hodd ? lo : hi) + 1e-6f);
#pragma unroll
    for (int r = 0; r < 16; ++r) num[r] *= rd;
#pragma unroll
    for (int r = 0; r < 4; ++r) {
      num[r] = (hodd && hh == 0) ? 0.f : num[r];
      num[12 + r] = (!hodd && hh == 1) ? 0.f : num[12 + r];
    }
    st_sc1(rs, pack8<0>(num), lane * 16, so_o);
    st_sc1(rs, pack8<1>(num), lane * 16, so_o + 1024);
    post(mysync, 3);
    set_flag(ws, fl(FL2 + m1 * 4 + w) + h * 8, tag, lane);
  }
  if (act35) {
    LANE_VARS(); O128_VARS();
    u32x2 xr[4];
    {
      const char* p = (const char*)x + (size_t)(ttp * 32 + t32) * (D_ * 2) + (jn * 64 + kh * 32 + 4 * hh) * 2;
#pragma unroll
      for (int g = 0; g < 4; ++g) xr[g] = *(const u32x2*)(p + 16 * g);
    }
    landed(I_WO + kh);
    bf16x8 w0[16], w1[16];
    {
      LAS char* wb = lds + slot_of(I_WO) + kh * SLOT;
#pragma unroll
      for (int i = 0; i < 16; ++i) {
        const int hd = i >> 1;
        LAS char* p = wb + (28 * hd - 4 * (hd & 1) + 16 * (i & 1)) * 128;
        w0[i] = tr_frag(p + o128a, p + o128a + 1024);
        w1[i] = tr_frag(p + o128b, p + o128b + 1024);
      }
    }
    f32x16 a0 = {}, a1 = {};
    LDS_DONE();
    release(I_WO); release(I_WO + 1);
    wait_flags(ws, fl(FL2 + ttp) + (kh * 8 + (lane & 7)) * 8, lane < 8, tag);
    add1(lbase + L_BUSY, 1);
    u32x4 ob[16];
    const unsigned src = WS_O + ((ttp * 16 + kh * 8) * 2) * 1024;
#pragma unroll
    for (int i = 0; i < LA3; ++i) { ob[i] = ld_sc1(rs, lane * 16, src + i * 1024); SB(); }
    const u32x4 sy = rd4(lds + L_SYNC);
    FENCE();
#pragma unroll
    for (int i = 0; i < 16; ++i) {
      a0 = MFMA(w0[i], asbf(ob[i]), a0);
      if (i + LA3 < 16) ob[i + LA3] = ld_sc1(rs, lane * 16, src + (i + LA3) * 1024);
      a1 = MFMA(w1[i], asbf(ob[i]), a1);
      FENCE();
    }
    if (min4(sy) < 3) spin4(lds + L_SYNC, 3);
    {
      const f32x16& snd = kh ? a0 : a1;
#pragma unroll
      for (int g = 0; g < 4; ++g)
        *(LAS f32x4*)(lds + L_RED + w * 4096 + g * 1024 + lane * 16) = (f32x4){snd[4 * g], snd[4 * g + 1], snd[4 * g + 2], snd[4 * g + 3]};
    }
    post(mysync, 4);
    f32x16 o = kh ? a1 : a0;
#pragma unroll
    for (int g = 0; g < 4; ++g) {
      o[4 * g] += bf_lo(xr[g][0]); o[4 * g + 1] += bf_hi(xr[g][0]); o[4 * g + 2] += bf_lo(xr[g][1]); o[4 * g + 3] += bf_hi(xr[g][1]);
    }
    spin1(lds + L_SYNC + 4 * (w ^ 1), 4);
    float s0 = 0.f, q0 = 0.f, s1 = 0.f, q1 = 0.f;
    u32x4 hv[2];
#pragma unroll
    for (int g = 0; g < 4; ++g) {
      const f32x4 r = *(LAS f32x4*)(lds + L_RED + (w ^ 1) * 4096 + g * 1024 + lane * 16);
      const float v0 = o[4 * g] + r[0], v1 = o[4 * g + 1] + r[1], v2 = o[4 * g + 2] + r[2], v3 = o[4 * g + 3] + r[3];
      hv[g >> 1][(g & 1) * 2] = pk2(v0, v1); hv[g >> 1][(g & 1) * 2 + 1] = pk2(v2, v3);
      s0 += v0 + v2; s1 += v1 + v3; q0 = fmaf(v0, v0, fmaf(v2, v2, q0)); q1 = fmaf(v1, v1, fmaf(v3, v3, q1));
    }
    st_sc1(rs, hv[0], lane * 16, so_h);
    st_sc1(rs, hv[1], lane * 16, so_h + 1024);
    const unsigned s = __builtin_bit_cast(unsigned, xsum(s0 + s1)), q = __builtin_bit_cast(unsigned, xsum(q0 + q1));
    if (hh == 0) st8_sc1(rs, (u32x2){s, q}, t32 * 8, so_s);
    set_flag(ws, fl(FL3 + ttp) + nt3 * 8, tag, lane);
    add1(lbase + L_BUSY, -1);
    FENCE();
  }
  if (act35) {
    u32x4 gb[56];
    const unsigned hso = WS_HH + (ttp * 112 + kh * 56) * 1024;
    unsigned l16 = __builtin_amdgcn_mbcnt_hi(~0u, __builtin_amdgcn_mbcnt_lo(~0u, 0u)) * 16;
    asm volatile("" : "+v"(l16));
    auto ldg = [&](int i) __attribute__((always_inline)) { gb[i] = ld_sc1(rs, l16, hso + p5_ftile(i) * 1024); };
   {
    LANE_VARS();
    LAS char* cur[4];
#pragma unroll
    for (int t = 0; t < 4; ++t)
      cur[t] = lds + slot_of(I_A) + (4 * hh + q4) * 128 + 8 * (p4 & 1) + blk * 14336 + (((2 * t + (p4 >> 1)) ^ wrow_swz(q4) ^ (2 * blk)) * 16);
    bf16x8 f[2][4];
    auto enter = [&](int e) {
#pragma unroll
      for (int t = 0; t < 4; ++t) { cur[t] += slot_of(e) - slot_of(e - 1); asm volatile("" : "+v"(cur[t])); }
    };
    auto pa = [&](int i, int t) { return cur[t] + (i % 7) * 2048; };
    landed(I_A);
#pragma unroll
    for (int i = 0; i < 2; ++i)
#pragma unroll
      for (int t = 0; t < 4; ++t) f[i][t] = tr_frag(pa(i, t), pa(i, t) + 1024);
    FENCE();
    u32x4 hb[28];
    f32x2 sp[7];
    float nmu4, k1, sg;
    wait_flags(ws, fl(FL3 + ttg) + (lane & 15) * 8, lane < 14, tag);
    auto ldh = [&](int i) { hb[i] = ld_sc1(rs, l16, WS_HB + (ttg * 28 + i) * 1024); };
#pragma unroll
    for (int i = 0; i < LA4; ++i) {
      ldh(i);
      SB();
      if (i == 3) {
#pragma unroll
        for (int k = 0; k < 7; ++k) sp[k] = ld8_sc1(rs, l16 >> 1, WS_ST + ttg * 14 * 256 + k * 512);
        SB();
      }
    }
    unsigned nl = 0, xr_ = 0;
    FENCE();
#pragma unroll
    for (int part = 0; part < 2; ++part) {
      f32x16 acc[4] = {};
#pragma unroll
      for (int i = 0; i < 28; ++i) {
        const int j = part * 28 + i + 2, b = i & 1;
        if (j < 56 && j % 7 == 0) { if (rfl(nl) < 4) landed(I_A + j / 7); enter(I_A + j / 7); FENCE(); }
#pragma unroll
        for (int t = 0; t < 4; ++t) {
          if (t == 3 && i % 7 == 6) { asm volatile("" ::"v"(f[b][3])); release(I_A + part * 4 + i / 7); }
          acc[t] = MFMA(f[b][t], asbf(hb[i]), acc[t]);
          if (j < 56) f[b][t] = tr_frag(pa(j, t), pa(j, t) + 1024);
          if (t == 3 && j < 49 && j % 7 == 5) nl = rd1(lds + L_CNT + 4 * (I_A + j / 7 + 1));
          if (t == 3 && i == 27) xr_ = rd1(lds + L_XRDY + 4 + 4 * part);
          if (part == 0 && t == 0 && i + LA4 < 28) ldh(i + LA4);
          SB();
        }
        FENCE();
        if (part == 0 && i == 6) {
          float s = 0.f, q = 0.f;
#pragma unroll
          for (int k = 0; k < 7; ++k) { s += sp[k][0]; q += sp[k][1]; }
          s = xsum(s); q = xsum(q);
          const float mu = s * (1.0f / D_), var = fmaxf(q * (1.0f / D_) - mu * mu, 0.f) + 1e-5f, rs_ = __builtin_amdgcn_rsqf(var);
          nmu4 = -mu; k1 = -LOG2E * rs_; sg = var * rs_;
          asm volatile("" : "+v"(nmu4), "+v"(k1), "+v"(sg));
          FENCE();
        }
        if (part == 1 && i == 4) set_flag(ws, fl(FL4 + ttg) + j4 * 8, tag, lane);
        if (part == 1 && i == 27 - EARLY5) wait_flags(ws, fl(FL4 + ttp) + (kh * 7 + (lane & 7)) * 8, lane < 7, tag);
        if (part == 1 && i > 27 - EARLY5) { ldg(i - (28 - EARLY5)); FENCE(); }
      }
      if (rfl(xr_) < 4) spin1(lds + L_XRDY + 4 + 4 * part, 4);
      FENCE();
#pragma unroll
      for (int t = 0; t < 4; ++t) {
        st_sc1(rs, glu_tile(acc[t], lds + L_CS4 + ((part * 4 + t) * 32 + 4 * hh) * 4, nmu4, k1, sg), l16, so_g + (part * 4 + t) * 1024);
        if (t & 1) FENCE();
      }
      FENCE();
    }
    set_flag(ws, fl(FL4 + 64 + ttg) + j4 * 8, tag, lane);
   }
    LANE_VARS(); O128_VARS();
    const unsigned f4 = fl(FL4 + ttp) + (kh * 7 + (lane & 7)) * 8;
    constexpr int PD = 4;
    bf16x8 f0[PD], f1[PD];
    auto pd = [&](int i) { return lds + slot_of(I_D + i / 7) + (kh * 14 + 2 * (i % 7)) * 1024; };
    landed(I_D);
#pragma unroll
    for (int i = 0; i < PD; ++i) { f0[i] = tr_frag(pd(i) + o128a, pd(i) + o128a + 1024); f1[i] = tr_frag(pd(i) + o128b, pd(i) + o128b + 1024); }
    FENCE();
#pragma unroll
    for (int i = EARLY5; i < LA5; ++i) { ldg(i); SB(); }
    unsigned nl = rd1(lds + L_CNT + 4 * (I_D + 1));
    FENCE();
    u32x4 res[2];
    f32x16 a0 = {}, a1 = {};
    static_for<56>([&](auto ic) __attribute__((always_inline)) {
      constexpr int i = decltype(ic)::value, j = i + PD, b = i % PD;
      if (i == WB5) {
        wait_flags(ws, f4 + 64 * FSTRIDE, lane < 7, tag);
      }
      if (i == 44) {
        res[0] = ld_sc1(rs, l16, WS_HB + (ttp * 28 + (jn * 2 + kh) * 2) * 1024);
        res[1] = ld_sc1(rs, l16 + 1024, WS_HB + (ttp * 28 + (jn * 2 + kh) * 2) * 1024);
        FENCE();
      }
      if (j < 56 && j % 7 == 0) { if (rfl(nl) < 4) landed(I_D + j / 7); FENCE(); }
      a0 = MFMA(f0[b], asbf(gb[i]), a0);
      if constexpr (j < 56) f0[b] = tr_frag(pd(j) + o128a, pd(j) + o128a + 1024);
      if constexpr (i + LA5 < 28) ldg(i + LA5);
      if constexpr (i >= WB5 && 28 + 2 * (i - WB5) < 56) { ldg(28 + 2 * (i - WB5)); ldg(29 + 2 * (i - WB5)); }
      SB();
      if (i % 7 == 6 && i < 21) { asm volatile("" ::"v"(f1[b])); release(I_D + i / 7); }
      a1 = MFMA(f1[b], asbf(gb[i]), a1);
      if constexpr (j < 56) f1[b] = tr_frag(pd(j) + o128b, pd(j) + o128b + 1024);
      if (j < 49 && j % 7 == 4 && j > 7) nl = rd1(lds + L_CNT + 4 * (I_D + j / 7 + 1));
      FENCE();
    });
    {
      const f32x16& snd = kh ? a0 : a1;
#pragma unroll
      for (int g = 0; g < 4; ++g)
        *(LAS f32x4*)(lds + L_RED + w * 4096 + g * 1024 + lane * 16) = (f32x4){snd[4 * g], snd[4 * g + 1], snd[4 * g + 2], snd[4 * g + 3]};
    }
    post(mysync, 5);
    f32x16 o = kh ? a1 : a0;
    spin1(lds + L_SYNC + 4 * (w ^ 1), 5);
    char* op = (char*)out + (size_t)(ttp * 32 + t32) * (D_ * 2) + (jn * 64 + kh * 32 + 8 * hh) * 2;
#pragma unroll
    for (int k = 0; k < 2; ++k) {
      const f32x4 r0 = *(LAS f32x4*)(lds + L_RED + (w ^ 1) * 4096 + (2 * k) * 1024 + lane * 16);
      const f32x4 r1 = *(LAS f32x4*)(lds + L_RED + (w ^ 1) * 4096 + (2 * k + 1) * 1024 + lane * 16);
      const u32x4 r = res[k];
      unsigned a_x = pk2(o[8 * k] + r0[0] + bf_lo(r[0]), o[8 * k + 1] + r0[1] + bf_hi(r[0]));
      unsigned a_y = pk2(o[8 * k + 2] + r0[2] + bf_lo(r[1]), o[8 * k + 3] + r0[3] + bf_hi(r[1]));
      unsigned b_x = pk2(o[8 * k + 4] + r1[0] + bf_lo(r[2]), o[8 * k + 5] + r1[1] + bf_hi(r[2]));
      unsigned b_y = pk2(o[8 * k + 6] + r1[2] + bf_lo(r[3]), o[8 * k + 7] + r1[3] + bf_hi(r[3]));
      auto s0 = __builtin_amdgcn_permlane32_swap(a_x, b_x, false, false);
      auto s1 = __builtin_amdgcn_permlane32_swap(a_y, b_y, false, false);
      *(u32x4*)(op + 32 * k) = (u32x4){s0[0], s1[0], s0[1], s1[1]};
    }
    if (bid == 0 && w == 0 && lane == 0) __hip_atomic_store((GAS u64*)(ws + WS_E), tag, RLX_AGENT);
  }
}

extern "C" void kernel_launch(void* const* d_in, const int* in_sizes, int n_in, void* d_out, int out_size, void* d_ws,
                              size_t ws_size, hipStream_t stream) {
  (void)hipFuncSetAttribute((const void*)fused_block, hipFuncAttributeMaxDynamicSharedMemorySize, LDS_BYTES);
  fused_block<<<256, 512, LDS_BYTES, stream>>>((const __bf16*)d_in[0], (const __bf16*)d_in[1], (const __bf16*)d_in[2],
                                               (const __bf16*)d_in[3], (const __bf16*)d_in[5], (const __bf16*)d_in[6],
                                               (const __bf16*)d_in[7], (char*)d_ws, (__bf16*)d_out);
}
```
